```python
import torch

def _softmax(x, axis=-1):
    return torch.softmax(x, dim=axis)
def _gelu(x):
    return torch.nn.functional.gelu(x, approximate="tanh")
def _silu(x):
    return torch.nn.functional.silu(x)
def _sigmoid(x):
    return torch.sigmoid(x)
def _layernorm(x, axis=-1, eps=1e-5):
    m = x.mean(dim=axis, keepdim=True)
    v = x.var(dim=axis, keepdim=True, unbiased=False)
    return (x - m) / torch.sqrt(v + eps)
def _scatter_add(tgt, idx, src, axis=0):
    return tgt.clone().index_add_(axis, idx, src)
def _segment_sum(src, seg, n):
    out = torch.zeros((n,) + src.shape[1:], dtype=src.dtype)
    return out.index_add_(0, seg, src)
def _pad(x, pw):
    flat = tuple(v for pair in reversed(pw) for v in pair)
    return torch.nn.functional.pad(x, flat)

kernel_name = 'concat_heads__mha_block_cached__gqa_block_cached__decode'

def setup_inputs():

    _w = (lambda a: (lambda t: t.to(torch.bfloat16) if t.is_floating_point() else t)(torch.as_tensor(a)))
    def _A_setup():
        B, Sq, D = 1, 1, 512
        H, Sk, Dh = 32, 8192, 128
        return {
            "x": _w((lambda *s: torch.randn(*s, dtype=torch.bfloat16) * 0.8)(B, Sq, D)),
            "k_cache": _w((lambda *s: torch.randn(*s, dtype=torch.bfloat16) * 0.8)(B, H, Sk, Dh)),
            "v_cache": _w((lambda *s: torch.randn(*s, dtype=torch.bfloat16) * 0.05)(B, H, Sk, Dh)),
            "wq": _w((lambda *s: torch.randn(*s, dtype=torch.bfloat16) * 0.8)(D, H * Dh) * (D ** -0.5)),
            "wo": _w((lambda *s: torch.randn(*s, dtype=torch.bfloat16) * 0.05)(H * Dh, D) * ((H * Dh) ** -0.5)),
        }
    _A_in = _A_setup()

    def _B_setup():
        B, Sq, D = 1, 1, 512
        Hq, Hkv, Sk, Dh = 32, 32, 8192, 128
        assert Hq % Hkv == 0, (Hq, Hkv)
        return {
            "x": _w((lambda *s: torch.randn(*s, dtype=torch.bfloat16) * 0.8)(B, Sq, D)),
            "k_cache": _w((lambda *s: torch.randn(*s, dtype=torch.bfloat16) * 0.8)(B, Hkv, Sk, Dh)),
            "v_cache": _w((lambda *s: torch.randn(*s, dtype=torch.bfloat16) * 0.05)(B, Hkv, Sk, Dh)),
            "wq": _w((lambda *s: torch.randn(*s, dtype=torch.bfloat16) * 0.8)(D, Hq * Dh) * (D ** -0.5)),
            "wo": _w((lambda *s: torch.randn(*s, dtype=torch.bfloat16) * 0.05)(Hq * Dh, D) * ((Hq * Dh) ** -0.5)),
        }
    _B_in = _B_setup()

    return {
        "A_x": _A_in["x"],
        "A_k_cache": _A_in["k_cache"],
        "A_v_cache": _A_in["v_cache"],
        "A_wq": _A_in["wq"],
        "A_wo": _A_in["wo"],
        "B_x": _B_in["x"],
        "B_k_cache": _B_in["k_cache"],
        "B_v_cache": _B_in["v_cache"],
        "B_wq": _B_in["wq"],
        "B_wo": _B_in["wo"],
    }

def reference(A_x, A_k_cache, A_v_cache, A_wq, A_wo, B_x, B_k_cache, B_v_cache, B_wq, B_wo):
    def _A_ref(x, k_cache, v_cache, wq, wo):
        B, Sq, D = x.shape
        H, Dh = 32, 128
        q = (lambda x, *d: x.permute(*d))((x @ wq).reshape(B, Sq, H, Dh), 0, 2, 1, 3)
        scores = torch.einsum("bhqd,bhkd->bhqk", q, k_cache) * (Dh ** -0.5)
        p = _softmax(scores, axis=-1)
        o = torch.einsum("bhqk,bhkd->bhqd", p, v_cache)
        return (lambda x, *d: x.permute(*d))(o, 0, 2, 1, 3).reshape(B, Sq, H * Dh) @ wo

    def _B_ref(x, k_cache, v_cache, wq, wo):
        B, Sq, D = x.shape
        Hq, Dh = 32, 128
        Hkv = k_cache.shape[1]
        rep = Hq // Hkv
        q = (lambda x, *d: x.permute(*d))((x @ wq).reshape(B, Sq, Hq, Dh), 0, 2, 1, 3)
        k = (lambda x, n, axis: torch.repeat_interleave(x, n, dim=axis))(k_cache, rep, axis=1)
        v = (lambda x, n, axis: torch.repeat_interleave(x, n, dim=axis))(v_cache, rep, axis=1)
        scores = torch.einsum("bhqd,bhkd->bhqk", q, k) * (Dh ** -0.5)
        p = _softmax(scores, axis=-1)
        o = torch.einsum("bhqk,bhkd->bhqd", p, v)
        return (lambda x, *d: x.permute(*d))(o, 0, 2, 1, 3).reshape(B, Sq, Hq * Dh) @ wo

    a = _A_ref(A_x, A_k_cache, A_v_cache, A_wq, A_wo)
    b = _B_ref(B_x, B_k_cache, B_v_cache, B_wq, B_wo)
    return torch.concatenate([a, b], axis=-1)
```

```cpp
#include <hip/hip_runtime.h>
#include <hip/hip_bf16.h>

#define WIN 32
#define KV_NT 1

namespace {

typedef unsigned u32x4 __attribute__((ext_vector_type(4)));
typedef float f32x4 __attribute__((ext_vector_type(4)));
typedef __bf16 bf16x2 __attribute__((ext_vector_type(2)));
#define GAS __attribute__((address_space(1)))

constexpr int DM = 512;
constexpr int NH = 32;
constexpr int SK = 8192;
constexpr int DH = 128;
constexpr int NSPLIT = 4;
constexpr int NWAVE = 8;
constexpr int NTHR = NWAVE * 64;
constexpr int KEYS_WG = SK / NSPLIT;
constexpr int KEYS_WAVE = KEYS_WG / NWAVE;
constexpr int NKL = KEYS_WAVE / 4;
constexpr int NQL = 16;
constexpr int NOL = 16;
constexpr int NSTREAM = NQL + NKL + NKL + NOL;

__device__ __forceinline__ float dot2(unsigned k, unsigned q, float acc) {
  return __builtin_amdgcn_fdot2_f32_bf16(__builtin_bit_cast(bf16x2, k), __builtin_bit_cast(bf16x2, q), acc, false);
}
template <int CTRL> __device__ __forceinline__ float dpp(float x) {
  return __builtin_bit_cast(float, __builtin_amdgcn_mov_dpp(__builtin_bit_cast(int, x), CTRL, 0xf, 0xf, true));
}
constexpr int XOR1 = 0xB1, XOR2 = 0x4E, XOR7 = 0x141, XOR8 = 0x128;
__device__ __forceinline__ float xrow16_max(float x) {
  auto s = __builtin_amdgcn_permlane16_swap(__float_as_uint(x), __float_as_uint(x), false, false);
  x = fmaxf(__uint_as_float(s[0]), __uint_as_float(s[1]));
  auto t = __builtin_amdgcn_permlane32_swap(__float_as_uint(x), __float_as_uint(x), false, false);
  return fmaxf(__uint_as_float(t[0]), __uint_as_float(t[1]));
}
__device__ __forceinline__ float xrow16_sum(float x) {
  auto s = __builtin_amdgcn_permlane16_swap(__float_as_uint(x), __float_as_uint(x), false, false);
  x = __uint_as_float(s[0]) + __uint_as_float(s[1]);
  auto t = __builtin_amdgcn_permlane32_swap(__float_as_uint(x), __float_as_uint(x), false, false);
  return __uint_as_float(t[0]) + __uint_as_float(t[1]);
}
__device__ __forceinline__ float row16_max(float x) {
  x = fmaxf(x, dpp<XOR1>(x)); x = fmaxf(x, dpp<XOR2>(x)); x = fmaxf(x, dpp<XOR7>(x)); x = fmaxf(x, dpp<XOR8>(x));
  return x;
}
__device__ __forceinline__ float row16_sum(float x) {
  x += dpp<XOR1>(x); x += dpp<XOR2>(x); x += dpp<XOR7>(x); x += dpp<XOR8>(x);
  return x;
}
__device__ __forceinline__ float bflo(unsigned u) { return __builtin_bit_cast(float, u << 16); }
__device__ __forceinline__ float bfhi(unsigned u) { return __builtin_bit_cast(float, u & 0xffff0000u); }
__device__ __forceinline__ unsigned cvtpk(float lo, float hi) {
  unsigned r; asm volatile("v_cvt_pk_bf16_f32 %0, %1, %2" : "=v"(r) : "v"(lo), "v"(hi)); return r;
}
__device__ __forceinline__ u32x4 ld_nt(const GAS u32x4* p) {
  return __builtin_nontemporal_load(p);
}

template <int T> __device__ __forceinline__ float bcast16(float x) { return dpp<0x150 + T>(x); }

constexpr size_t WS_Y = 0;
constexpr size_t WS_ML = (size_t)256 * 512 * 4;

__global__ void __launch_bounds__(NTHR, 2)
attn_main(const unsigned short* __restrict__ xA, const unsigned short* __restrict__ kA, const unsigned short* __restrict__ vA,
          const unsigned short* __restrict__ wqA, const unsigned short* __restrict__ woA,
          const unsigned short* __restrict__ xB, const unsigned short* __restrict__ kB, const unsigned short* __restrict__ vB,
          const unsigned short* __restrict__ wqB, const unsigned short* __restrict__ woB,
          float* __restrict__ ws) {
  __shared__ __attribute__((aligned(16))) float lds[8 * 128 + 8 * 128 + 32 + 8 * 512];
  float* l_q = lds;
  float* l_o = lds + 1024;
  float* l_ml = lds + 2048;
  float* l_y = lds + 2048 + 32;

  const int b = blockIdx.x;
  const int idx = b >> 3;
  const int hg = (b & 7) * 8 + (idx >> 2);
  const int split = idx & 3;
  const int prob = hg >> 5, h = hg & 31;
  const int tid = threadIdx.x, lane = tid & 63;
  const int wave = __builtin_amdgcn_readfirstlane(tid >> 6);
  const int r = lane >> 4, c = lane & 15;

  const unsigned short* x = prob ? xB : xA;
  const unsigned short* kc = prob ? kB : kA;
  const unsigned short* vc = prob ? vB : vA;
  const unsigned short* wq = prob ? wqB : wqA;
  const unsigned short* wo = prob ? woB : woA;

  const GAS u32x4* pq = (const GAS u32x4*)(wq + (size_t)(wave * 64 + r * 16) * (NH * DH) + h * DH + 8 * c);
  const size_t kvoff = ((size_t)h * SK + (size_t)split * KEYS_WG + (size_t)wave * KEYS_WAVE) * DH + (size_t)lane * 8;
  const GAS u32x4* pk = (const GAS u32x4*)(kc + kvoff);
  const GAS u32x4* pv = (const GAS u32x4*)(vc + kvoff);
  const GAS u32x4* po = (const GAS u32x4*)(wo + (size_t)(h * DH + wave * 16) * DM + 8 * lane);

  const GAS u32x4* px = (const GAS u32x4*)(x + wave * 64 + r * 16);
  u32x4 xv0 = px[0], xv1 = px[1];

  u32x4 buf[WIN];
  auto issue = [&](auto I_) {
    constexpr int I = decltype(I_)::value;
    if constexpr (I < NQL) buf[I % WIN] = pq[(size_t)I * (NH * DH / 8)];
    else if constexpr (I < NQL + NKL) buf[I % WIN] = ld_nt(pk + (I - NQL) * 64);
    else if constexpr (I < NQL + 2 * NKL) buf[I % WIN] = ld_nt(pv + (I - NQL - NKL) * 64);
    else if constexpr (I < NSTREAM) buf[I % WIN] = po[(I - NQL - 2 * NKL) * (DM / 8)];
  };
#define STATIC_FOR(N, ...) [&]<int... Is>(std::integer_sequence<int, Is...>) { ([&](auto I_) __VA_ARGS__ (std::integral_constant<int, Is>{}), ...); }(std::make_integer_sequence<int, N>{})

  STATIC_FOR(WIN, { issue(I_); });
  __builtin_amdgcn_sched_barrier(0);

  float qa[8];
#pragma unroll
  for (int k = 0; k < 8; ++k) qa[k] = 0.f;
  STATIC_FOR(NQL, {
    constexpr int I = decltype(I_)::value;
    const u32x4 w = buf[I % WIN];
    const unsigned xw = (I < 8) ? xv0[(I & 7) >> 1] : xv1[(I & 7) >> 1];
    const float xs = (I & 1) ? bfhi(xw) : bflo(xw);
    _Pragma("unroll") for (int k = 0; k < 4; ++k) {
      qa[2 * k] = fmaf(xs, bflo(w[k]), qa[2 * k]);
      qa[2 * k + 1] = fmaf(xs, bfhi(w[k]), qa[2 * k + 1]);
    }
    issue(std::integral_constant<int, I + WIN>{});
    __builtin_amdgcn_sched_barrier(0);
  });
#pragma unroll
  for (int k = 0; k < 8; ++k) qa[k] = xrow16_sum(qa[k]);
  if (r == 0) {
    *(f32x4*)(l_q + wave * 128 + 8 * c) = f32x4{qa[0], qa[1], qa[2], qa[3]};
    *(f32x4*)(l_q + wave * 128 + 8 * c + 4) = f32x4{qa[4], qa[5], qa[6], qa[7]};
  }
  __syncthreads();
  u32x4 qp;
  {
    f32x4 a = {0.f, 0.f, 0.f, 0.f}, bb = {0.f, 0.f, 0.f, 0.f};
#pragma unroll
    for (int w = 0; w < NWAVE; ++w) {
      a += *(const f32x4*)(l_q + w * 128 + 8 * c);
      bb += *(const f32x4*)(l_q + w * 128 + 8 * c + 4);
    }
    qp[0] = cvtpk(a[0], a[1]); qp[1] = cvtpk(a[2], a[3]); qp[2] = cvtpk(bb[0], bb[1]); qp[3] = cvtpk(bb[2], bb[3]);
  }
  __builtin_amdgcn_sched_barrier(0);

  float sc[4];
  float xs[16];
  STATIC_FOR(NKL, {
    constexpr int J = decltype(I_)::value;
    constexpr int I = NQL + J;
    const u32x4 kk = buf[I % WIN];
    float d = dot2(kk[0], qp[0], 0.f);
    d = dot2(kk[1], qp[1], d); d = dot2(kk[2], qp[2], d); d = dot2(kk[3], qp[3], d);
    xs[J & 15] = d;
    issue(std::integral_constant<int, I + WIN>{});
    if constexpr ((J & 15) == 15) {
      _Pragma("unroll") for (int hh = 0; hh < 8; ++hh) { const bool up = (c & 8); const float keep = up ? xs[hh + 8] : xs[hh], send = up ? xs[hh] : xs[hh + 8]; xs[hh] = keep + dpp<XOR8>(send); }
      _Pragma("unroll") for (int hh = 0; hh < 4; ++hh) { const bool up = (c & 4); const float keep = up ? xs[hh + 4] : xs[hh], send = up ? xs[hh] : xs[hh + 4]; xs[hh] = keep + dpp<XOR7>(send); }
      _Pragma("unroll") for (int hh = 0; hh < 2; ++hh) { const bool up = (c & 2); const float keep = up ? xs[hh + 2] : xs[hh], send = up ? xs[hh] : xs[hh + 2]; xs[hh] = keep + dpp<XOR2>(send); }
      { const bool up = (c & 1); const float keep = up ? xs[1] : xs[0], send = up ? xs[0] : xs[1]; xs[0] = keep + dpp<XOR1>(send); }
      sc[J >> 4] = xs[0];
    }
    __builtin_amdgcn_sched_barrier(0);
  });

  constexpr float C2 = 0.08838834764831845f * 1.4426950408889634f;
  float m = fmaxf(fmaxf(sc[0], sc[1]), fmaxf(sc[2], sc[3]));
  m = xrow16_max(row16_max(m));
  m *= C2;
  float p[4];
#pragma unroll
  for (int g = 0; g < 4; ++g) p[g] = __builtin_amdgcn_exp2f(fmaf(sc[g], C2, -m));
  float l = (p[0] + p[1]) + (p[2] + p[3]);
  l = xrow16_sum(row16_sum(l));
  __builtin_amdgcn_sched_barrier(0);

  float acc[8];
#pragma unroll
  for (int k = 0; k < 8; ++k) acc[k] = 0.f;
  STATIC_FOR(NKL, {
    constexpr int J = decltype(I_)::value;
    constexpr int I = NQL + NKL + J;
    const u32x4 vv = buf[I % WIN];
    const float pb = bcast16<J & 15>(p[J >> 4]);
    _Pragma("unroll") for (int k = 0; k < 4; ++k) {
      acc[2 * k] = fmaf(pb, bflo(vv[k]), acc[2 * k]);
      acc[2 * k + 1] = fmaf(pb, bfhi(vv[k]), acc[2 * k + 1]);
    }
    issue(std::integral_constant<int, I + WIN>{});
    __builtin_amdgcn_sched_barrier(0);
  });
#pragma unroll
  for (int k = 0; k < 8; ++k) acc[k] = xrow16_sum(acc[k]);
  if (r == 0) {
    *(f32x4*)(l_o + wave * 128 + 8 * c) = f32x4{acc[0], acc[1], acc[2], acc[3]};
    *(f32x4*)(l_o + wave * 128 + 8 * c + 4) = f32x4{acc[4], acc[5], acc[6], acc[7]};
    if (c == 0) { l_ml[2 * wave] = m; l_ml[2 * wave + 1] = l; }
  }
  __syncthreads();
  float M = l_ml[0];
#pragma unroll
  for (int w = 1; w < NWAVE; ++w) M = fmaxf(M, l_ml[2 * w]);
  float L = 0.f;
#pragma unroll
  for (int w = 0; w < NWAVE; ++w) L = fmaf(__builtin_amdgcn_exp2f(l_ml[2 * w] - M), l_ml[2 * w + 1], L);
  float om;
  {
    const float s0 = __builtin_amdgcn_exp2f(l_ml[2 * (2 * r)] - M), s1 = __builtin_amdgcn_exp2f(l_ml[2 * (2 * r + 1)] - M);
    om = s0 * l_o[(2 * r) * 128 + wave * 16 + c] + s1 * l_o[(2 * r + 1) * 128 + wave * 16 + c];
    om = xrow16_sum(om);
  }
  float y[8];
#pragma unroll
  for (int k = 0; k < 8; ++k) y[k] = 0.f;
  STATIC_FOR(NOL, {
    constexpr int J = decltype(I_)::value;
    constexpr int I = NQL + 2 * NKL + J;
    const u32x4 w = buf[I % WIN];
    const float ob = bcast16<J>(om);
    _Pragma("unroll") for (int k = 0; k < 4; ++k) {
      y[2 * k] = fmaf(ob, bflo(w[k]), y[2 * k]);
      y[2 * k + 1] = fmaf(ob, bfhi(w[k]), y[2 * k + 1]);
    }
  });
  *(f32x4*)(l_y + wave * 512 + 8 * lane) = f32x4{y[0], y[1], y[2], y[3]};
  *(f32x4*)(l_y + wave * 512 + 8 * lane + 4) = f32x4{y[4], y[5], y[6], y[7]};
  __syncthreads();
  float yy = 0.f;
#pragma unroll
  for (int w = 0; w < NWAVE; ++w) yy += l_y[w * 512 + tid];
  const int slot = hg * NSPLIT + split;
  ws[WS_Y / 4 + (size_t)slot * 512 + tid] = yy;
  if (tid == 0) { ws[WS_ML / 4 + 2 * slot] = M; ws[WS_ML / 4 + 2 * slot + 1] = L; }
}

__global__ void __launch_bounds__(256)
attn_combine(const float* __restrict__ ws, __hip_bfloat16* __restrict__ out) {
  __shared__ float wgt[128];
  __shared__ float red[8][32];
  const int prob = blockIdx.x >> 4, cb = blockIdx.x & 15;
  const int tid = threadIdx.x, g = tid >> 5, col = cb * 32 + (tid & 31);
  float yv[16];
#pragma unroll
  for (int i = 0; i < 16; ++i) yv[i] = ws[WS_Y / 4 + (size_t)(prob * 128 + g * 16 + i) * 512 + col];
  if (tid < 128) {
    const float m = ws[WS_ML / 4 + 2 * (prob * 128 + tid)], l = ws[WS_ML / 4 + 2 * (prob * 128 + tid) + 1];
    float M = fmaxf(m, dpp<XOR1>(m)); M = fmaxf(M, dpp<XOR2>(M));
    const float w = __builtin_amdgcn_exp2f(m - M);
    float L = w * l; L += dpp<XOR1>(L); L += dpp<XOR2>(L);
    wgt[tid] = w / L;
  }
  __syncthreads();
  float s = 0.f;
#pragma unroll
  for (int i = 0; i < 16; ++i) s = fmaf(wgt[g * 16 + i], yv[i], s);
  red[g][tid & 31] = s;
  __syncthreads();
  if (tid < 32) {
    float t = 0.f;
#pragma unroll
    for (int i = 0; i < 8; ++i) t += red[i][tid];
    out[prob * 512 + cb * 32 + tid] = __float2bfloat16(t);
  }
}

}

extern "C" void kernel_launch(void* const* d_in, const int* in_sizes, int n_in,
                              void* d_out, int out_size, void* d_ws, size_t ws_size,
                              hipStream_t stream) {
  attn_main<<<256, NTHR, 0, stream>>>(
      (const unsigned short*)d_in[0], (const unsigned short*)d_in[1], (const unsigned short*)d_in[2],
      (const unsigned short*)d_in[3], (const unsigned short*)d_in[4],
      (const unsigned short*)d_in[5], (const unsigned short*)d_in[6], (const unsigned short*)d_in[7],
      (const unsigned short*)d_in[8], (const unsigned short*)d_in[9], (float*)d_ws);
  attn_combine<<<32, 256, 0, stream>>>((const float*)d_ws, (__hip_bfloat16*)d_out);
}
```

```cpp
#include <hip/hip_runtime.h>
#include <hip/hip_bf16.h>

#define WIN 32
#define KV_NT 1

#define STAMP(i)

namespace {

typedef unsigned u32x4 __attribute__((ext_vector_type(4)));
typedef float f32x4 __attribute__((ext_vector_type(4)));
typedef __bf16 bf16x2 __attribute__((ext_vector_type(2)));
#define GAS __attribute__((address_space(1)))

constexpr int DM = 512;
constexpr int NH = 32;
constexpr int SK = 8192;
constexpr int DH = 128;
constexpr int NSPLIT = 4;
constexpr int NWAVE = 8;
constexpr int NTHR = NWAVE * 64;
constexpr int KEYS_WG = SK / NSPLIT;
constexpr int KEYS_WAVE = KEYS_WG / NWAVE;
constexpr int NKL = KEYS_WAVE / 4;
constexpr int NQL = 16;
constexpr int NOL = 16;
constexpr int NSTREAM = NQL + NKL + NKL + NOL;

__device__ __forceinline__ float dot2(unsigned k, unsigned q, float acc) {
  return __builtin_amdgcn_fdot2_f32_bf16(__builtin_bit_cast(bf16x2, k), __builtin_bit_cast(bf16x2, q), acc, false);
}
template <int CTRL> __device__ __forceinline__ float dpp(float x) {
  return __builtin_bit_cast(float, __builtin_amdgcn_mov_dpp(__builtin_bit_cast(int, x), CTRL, 0xf, 0xf, true));
}
constexpr int XOR1 = 0xB1, XOR2 = 0x4E, XOR7 = 0x141, XOR8 = 0x128;
__device__ __forceinline__ float xrow16_max(float x) {
  auto s = __builtin_amdgcn_permlane16_swap(__float_as_uint(x), __float_as_uint(x), false, false);
  x = fmaxf(__uint_as_float(s[0]), __uint_as_float(s[1]));
  auto t = __builtin_amdgcn_permlane32_swap(__float_as_uint(x), __float_as_uint(x), false, false);
  return fmaxf(__uint_as_float(t[0]), __uint_as_float(t[1]));
}
__device__ __forceinline__ float xrow16_sum(float x) {
  auto s = __builtin_amdgcn_permlane16_swap(__float_as_uint(x), __float_as_uint(x), false, false);
  x = __uint_as_float(s[0]) + __uint_as_float(s[1]);
  auto t = __builtin_amdgcn_permlane32_swap(__float_as_uint(x), __float_as_uint(x), false, false);
  return __uint_as_float(t[0]) + __uint_as_float(t[1]);
}
__device__ __forceinline__ float row16_max(float x) {
  x = fmaxf(x, dpp<XOR1>(x)); x = fmaxf(x, dpp<XOR2>(x)); x = fmaxf(x, dpp<XOR7>(x)); x = fmaxf(x, dpp<XOR8>(x));
  return x;
}
__device__ __forceinline__ float row16_sum(float x) {
  x += dpp<XOR1>(x); x += dpp<XOR2>(x); x += dpp<XOR7>(x); x += dpp<XOR8>(x);
  return x;
}
__device__ __forceinline__ float bflo(unsigned u) { return __builtin_bit_cast(float, u << 16); }
__device__ __forceinline__ float bfhi(unsigned u) { return __builtin_bit_cast(float, u & 0xffff0000u); }
__device__ __forceinline__ unsigned cvtpk(float lo, float hi) {
  unsigned r; asm volatile("v_cvt_pk_bf16_f32 %0, %1, %2" : "=v"(r) : "v"(lo), "v"(hi)); return r;
}
__device__ __forceinline__ u32x4 ld_nt(const GAS u32x4* p) {
  return __builtin_nontemporal_load(p);
}

#define W_NT 1
__device__ __forceinline__ u32x4 ld_w(const GAS u32x4* p) {
  return __builtin_nontemporal_load(p);
}
template <int T> __device__ __forceinline__ float bcast16(float x) { return dpp<0x150 + T>(x); }

constexpr size_t WS_Y = 0;
constexpr size_t WS_ML = (size_t)256 * 512 * 4;

__global__ void __launch_bounds__(NTHR, 2)
attn_main(const unsigned short* __restrict__ xA, const unsigned short* __restrict__ kA, const unsigned short* __restrict__ vA,
          const unsigned short* __restrict__ wqA, const unsigned short* __restrict__ woA,
          const unsigned short* __restrict__ xB, const unsigned short* __restrict__ kB, const unsigned short* __restrict__ vB,
          const unsigned short* __restrict__ wqB, const unsigned short* __restrict__ woB,
          float* __restrict__ ws) {
  __shared__ __attribute__((aligned(16))) float lds[8 * 128 + 8 * 128 + 32 + 8 * 512];
  STAMP(0);
  float* l_q = lds;
  float* l_o = lds + 1024;
  float* l_ml = lds + 2048;
  float* l_y = lds + 2048 + 32;

  const int b = blockIdx.x;
  const int idx = b >> 3;
  const int hg = (b & 7) * 8 + (idx >> 2);
  const int split = idx & 3;
  const int prob = hg >> 5, h = hg & 31;
  const int tid = threadIdx.x, lane = tid & 63;
  const int wave = __builtin_amdgcn_readfirstlane(tid >> 6);
  const int r = lane >> 4, c = lane & 15;

  const unsigned short* x = prob ? xB : xA;
  const unsigned short* kc = prob ? kB : kA;
  const unsigned short* vc = prob ? vB : vA;
  const unsigned short* wq = prob ? wqB : wqA;
  const unsigned short* wo = prob ? woB : woA;

  const GAS u32x4* pq = (const GAS u32x4*)(wq + (size_t)(wave * 64 + r * 16) * (NH * DH) + h * DH + 8 * c);
  const size_t kvoff = ((size_t)h * SK + (size_t)split * KEYS_WG + (size_t)wave * KEYS_WAVE) * DH + (size_t)lane * 8;
  const GAS u32x4* pk = (const GAS u32x4*)(kc + kvoff);
  const GAS u32x4* pv = (const GAS u32x4*)(vc + kvoff);
  const GAS u32x4* po = (const GAS u32x4*)(wo + (size_t)(h * DH + wave * 16) * DM + 8 * lane);

  const GAS u32x4* px = (const GAS u32x4*)(x + wave * 64 + r * 16);
  u32x4 xv0 = px[0], xv1 = px[1];

  u32x4 buf[WIN];
  auto issue = [&](auto I_) {
    constexpr int I = decltype(I_)::value;
    if constexpr (I < NQL) buf[I % WIN] = ld_w(pq + (size_t)I * (NH * DH / 8));
    else if constexpr (I < NQL + NKL) buf[I % WIN] = ld_nt(pk + (I - NQL) * 64);
    else if constexpr (I < NQL + 2 * NKL) buf[I % WIN] = ld_nt(pv + (I - NQL - NKL) * 64);
    else if constexpr (I < NSTREAM) buf[I % WIN] = ld_w(po + (I - NQL - 2 * NKL) * (DM / 8));
  };
#define STATIC_FOR(N, ...) [&]<int... Is>(std::integer_sequence<int, Is...>) { ([&](auto I_) __VA_ARGS__ (std::integral_constant<int, Is>{}), ...); }(std::make_integer_sequence<int, N>{})

  STATIC_FOR(WIN, { issue(I_); });
  __builtin_amdgcn_sched_barrier(0);
  STAMP(1);

  float qa[8];
#pragma unroll
  for (int k = 0; k < 8; ++k) qa[k] = 0.f;
  STATIC_FOR(NQL, {
    constexpr int I = decltype(I_)::value;
    const u32x4 w = buf[I % WIN];
    const unsigned xw = (I < 8) ? xv0[(I & 7) >> 1] : xv1[(I & 7) >> 1];
    const float xs = (I & 1) ? bfhi(xw) : bflo(xw);
    _Pragma("unroll") for (int k = 0; k < 4; ++k) {
      qa[2 * k] = fmaf(xs, bflo(w[k]), qa[2 * k]);
      qa[2 * k + 1] = fmaf(xs, bfhi(w[k]), qa[2 * k + 1]);
    }
    issue(std::integral_constant<int, I + WIN>{});
    __builtin_amdgcn_sched_barrier(0);
  });
#pragma unroll
  for (int k = 0; k < 8; ++k) qa[k] = xrow16_sum(qa[k]);
  if (r == 0) {
    *(f32x4*)(l_q + wave * 128 + 8 * c) = f32x4{qa[0], qa[1], qa[2], qa[3]};
    *(f32x4*)(l_q + wave * 128 + 8 * c + 4) = f32x4{qa[4], qa[5], qa[6], qa[7]};
  }
  __syncthreads();
  u32x4 qp;
  {
    f32x4 a = {0.f, 0.f, 0.f, 0.f}, bb = {0.f, 0.f, 0.f, 0.f};
#pragma unroll
    for (int w = 0; w < NWAVE; ++w) {
      a += *(const f32x4*)(l_q + w * 128 + 8 * c);
      bb += *(const f32x4*)(l_q + w * 128 + 8 * c + 4);
    }
    qp[0] = cvtpk(a[0], a[1]); qp[1] = cvtpk(a[2], a[3]); qp[2] = cvtpk(bb[0], bb[1]); qp[3] = cvtpk(bb[2], bb[3]);
  }
  __builtin_amdgcn_sched_barrier(0);

  STAMP(2);
  float sc[4];
  float xs[16];
  STATIC_FOR(NKL, {
    constexpr int J = decltype(I_)::value;
    constexpr int I = NQL + J;
    const u32x4 kk = buf[I % WIN];
    float d = dot2(kk[0], qp[0], 0.f);
    d = dot2(kk[1], qp[1], d); d = dot2(kk[2], qp[2], d); d = dot2(kk[3], qp[3], d);
    xs[J & 15] = d;
    issue(std::integral_constant<int, I + WIN>{});
    if constexpr ((J & 15) == 15) {
      _Pragma("unroll") for (int hh = 0; hh < 8; ++hh) { const bool up = (c & 8); const float keep = up ? xs[hh + 8] : xs[hh], send = up ? xs[hh] : xs[hh + 8]; xs[hh] = keep + dpp<XOR8>(send); }
      _Pragma("unroll") for (int hh = 0; hh < 4; ++hh) { const bool up = (c & 4); const float keep = up ? xs[hh + 4] : xs[hh], send = up ? xs[hh] : xs[hh + 4]; xs[hh] = keep + dpp<XOR7>(send); }
      _Pragma("unroll") for (int hh = 0; hh < 2; ++hh) { const bool up = (c & 2); const float keep = up ? xs[hh + 2] : xs[hh], send = up ? xs[hh] : xs[hh + 2]; xs[hh] = keep + dpp<XOR2>(send); }
      { const bool up = (c & 1); const float keep = up ? xs[1] : xs[0], send = up ? xs[0] : xs[1]; xs[0] = keep + dpp<XOR1>(send); }
      sc[J >> 4] = xs[0];
    }
    __builtin_amdgcn_sched_barrier(0);
  });

  STAMP(3);
  constexpr float C2 = 0.08838834764831845f * 1.4426950408889634f;
  float m = fmaxf(fmaxf(sc[0], sc[1]), fmaxf(sc[2], sc[3]));
  m = xrow16_max(row16_max(m));
  m *= C2;
  float p[4];
#pragma unroll
  for (int g = 0; g < 4; ++g) p[g] = __builtin_amdgcn_exp2f(fmaf(sc[g], C2, -m));
  float l = (p[0] + p[1]) + (p[2] + p[3]);
  l = xrow16_sum(row16_sum(l));
  __builtin_amdgcn_sched_barrier(0);

  float acc[8];
#pragma unroll
  for (int k = 0; k < 8; ++k) acc[k] = 0.f;
  STATIC_FOR(NKL, {
    constexpr int J = decltype(I_)::value;
    constexpr int I = NQL + NKL + J;
    const u32x4 vv = buf[I % WIN];
    const float pb = bcast16<J & 15>(p[J >> 4]);
    _Pragma("unroll") for (int k = 0; k < 4; ++k) {
      acc[2 * k] = fmaf(pb, bflo(vv[k]), acc[2 * k]);
      acc[2 * k + 1] = fmaf(pb, bfhi(vv[k]), acc[2 * k + 1]);
    }
    issue(std::integral_constant<int, I + WIN>{});
    __builtin_amdgcn_sched_barrier(0);
  });
  STAMP(4);
#pragma unroll
  for (int k = 0; k < 8; ++k) acc[k] = xrow16_sum(acc[k]);
  if (r == 0) {
    *(f32x4*)(l_o + wave * 128 + 8 * c) = f32x4{acc[0], acc[1], acc[2], acc[3]};
    *(f32x4*)(l_o + wave * 128 + 8 * c + 4) = f32x4{acc[4], acc[5], acc[6], acc[7]};
    if (c == 0) { l_ml[2 * wave] = m; l_ml[2 * wave + 1] = l; }
  }
  __syncthreads();
  float M = l_ml[0];
#pragma unroll
  for (int w = 1; w < NWAVE; ++w) M = fmaxf(M, l_ml[2 * w]);
  float L = 0.f;
#pragma unroll
  for (int w = 0; w < NWAVE; ++w) L = fmaf(__builtin_amdgcn_exp2f(l_ml[2 * w] - M), l_ml[2 * w + 1], L);
  float om;
  {
    const float s0 = __builtin_amdgcn_exp2f(l_ml[2 * (2 * r)] - M), s1 = __builtin_amdgcn_exp2f(l_ml[2 * (2 * r + 1)] - M);
    om = s0 * l_o[(2 * r) * 128 + wave * 16 + c] + s1 * l_o[(2 * r + 1) * 128 + wave * 16 + c];
    om = xrow16_sum(om);
  }
  STAMP(5);
  float y[8];
#pragma unroll
  for (int k = 0; k < 8; ++k) y[k] = 0.f;
  STATIC_FOR(NOL, {
    constexpr int J = decltype(I_)::value;
    constexpr int I = NQL + 2 * NKL + J;
    const u32x4 w = buf[I % WIN];
    const float ob = bcast16<J>(om);
    _Pragma("unroll") for (int k = 0; k < 4; ++k) {
      y[2 * k] = fmaf(ob, bflo(w[k]), y[2 * k]);
      y[2 * k + 1] = fmaf(ob, bfhi(w[k]), y[2 * k + 1]);
    }
  });
  *(f32x4*)(l_y + wave * 512 + 8 * lane) = f32x4{y[0], y[1], y[2], y[3]};
  *(f32x4*)(l_y + wave * 512 + 8 * lane + 4) = f32x4{y[4], y[5], y[6], y[7]};
  __syncthreads();
  float yy = 0.f;
#pragma unroll
  for (int w = 0; w < NWAVE; ++w) yy += l_y[w * 512 + tid];
  const int slot = hg * NSPLIT + split;
  ws[WS_Y / 4 + (size_t)slot * 512 + tid] = yy;
  if (tid == 0) { ws[WS_ML / 4 + 2 * slot] = M; ws[WS_ML / 4 + 2 * slot + 1] = L; }
  STAMP(6);
}

__global__ void __launch_bounds__(256)
attn_combine(const float* __restrict__ ws, __hip_bfloat16* __restrict__ out) {
  __shared__ float wgt[128];
  __shared__ float red[8][32];
  const int prob = blockIdx.x >> 4, cb = blockIdx.x & 15;
  const int tid = threadIdx.x, g = tid >> 5, col = cb * 32 + (tid & 31);
  float yv[16];
#pragma unroll
  for (int i = 0; i < 16; ++i) yv[i] = ws[WS_Y / 4 + (size_t)(prob * 128 + g * 16 + i) * 512 + col];
  if (tid < 128) {
    const float m = ws[WS_ML / 4 + 2 * (prob * 128 + tid)], l = ws[WS_ML / 4 + 2 * (prob * 128 + tid) + 1];
    float M = fmaxf(m, dpp<XOR1>(m)); M = fmaxf(M, dpp<XOR2>(M));
    const float w = __builtin_amdgcn_exp2f(m - M);
    float L = w * l; L += dpp<XOR1>(L); L += dpp<XOR2>(L);
    wgt[tid] = w / L;
  }
  __syncthreads();
  float s = 0.f;
#pragma unroll
  for (int i = 0; i < 16; ++i) s = fmaf(wgt[g * 16 + i], yv[i], s);
  red[g][tid & 31] = s;
  __syncthreads();
  if (tid < 32) {
    float t = 0.f;
#pragma unroll
    for (int i = 0; i < 8; ++i) t += red[i][tid];
    out[prob * 512 + cb * 32 + tid] = __float2bfloat16(t);
  }
}

}

extern "C" void kernel_launch(void* const* d_in, const int* in_sizes, int n_in,
                              void* d_out, int out_size, void* d_ws, size_t ws_size,
                              hipStream_t stream) {
  attn_main<<<256, NTHR, 0, stream>>>(
      (const unsigned short*)d_in[0], (const unsigned short*)d_in[1], (const unsigned short*)d_in[2],
      (const unsigned short*)d_in[3], (const unsigned short*)d_in[4],
      (const unsigned short*)d_in[5], (const unsigned short*)d_in[6], (const unsigned short*)d_in[7],
      (const unsigned short*)d_in[8], (const unsigned short*)d_in[9], (float*)d_ws);
  attn_combine<<<32, 256, 0, stream>>>((const float*)d_ws, (__hip_bfloat16*)d_out);
}
```

```cpp
#include <hip/hip_runtime.h>
#include <hip/hip_bf16.h>
#include <utility>
#include <type_traits>

#define STAMP(i)

#define POLL_SLEEP 2

namespace {

typedef unsigned u32x4 __attribute__((ext_vector_type(4)));
typedef unsigned u32x2 __attribute__((ext_vector_type(2)));
typedef float f32x4 __attribute__((ext_vector_type(4)));
typedef __bf16 bf16x2 __attribute__((ext_vector_type(2)));
#define GAS __attribute__((address_space(1)))
typedef GAS unsigned gu32;
typedef GAS unsigned long long gu64;
#define RLX_AGENT __ATOMIC_RELAXED, __HIP_MEMORY_SCOPE_AGENT

constexpr int SK = 8192;
constexpr int NWAVE = 8;
constexpr int NTHR = NWAVE * 64;
#define QMODE 0
#define WO_IT 2
constexpr int NPO = 16;
constexpr int PAIR_B = 32768;
constexpr int AUX_NT = 2;
constexpr int AUX_SC1 = 16;
constexpr int RSRC_W3 = 0x00020000;

constexpr unsigned WS_CTL = 0;
constexpr unsigned WS_ML = 4096;
constexpr unsigned WS_Y = 65536;
constexpr unsigned WS_BYTES = WS_Y + 256 * 4096;

constexpr int LDS_FLOATS = 21 * 1024;

__device__ __forceinline__ float dot2(unsigned k, unsigned q, float acc) {
  return __builtin_amdgcn_fdot2_f32_bf16(__builtin_bit_cast(bf16x2, k), __builtin_bit_cast(bf16x2, q), acc, false);
}
template <int CTRL> __device__ __forceinline__ float dpp(float x) {
  return __builtin_bit_cast(float, __builtin_amdgcn_mov_dpp(__builtin_bit_cast(int, x), CTRL, 0xf, 0xf, true));
}
constexpr int XOR1 = 0xB1, XOR2 = 0x4E, XOR7 = 0x141, XOR8 = 0x128;
__device__ __forceinline__ float x16_max(float x) {
  auto s = __builtin_amdgcn_permlane16_swap(__float_as_uint(x), __float_as_uint(x), false, false);
  return fmaxf(__uint_as_float(s[0]), __uint_as_float(s[1]));
}
__device__ __forceinline__ float x32_max(float x) {
  auto t = __builtin_amdgcn_permlane32_swap(__float_as_uint(x), __float_as_uint(x), false, false);
  return fmaxf(__uint_as_float(t[0]), __uint_as_float(t[1]));
}
__device__ __forceinline__ float x16_sum(float x) {
  auto s = __builtin_amdgcn_permlane16_swap(__float_as_uint(x), __float_as_uint(x), false, false);
  return __uint_as_float(s[0]) + __uint_as_float(s[1]);
}
__device__ __forceinline__ float x32_sum(float x) {
  auto t = __builtin_amdgcn_permlane32_swap(__float_as_uint(x), __float_as_uint(x), false, false);
  return __uint_as_float(t[0]) + __uint_as_float(t[1]);
}
__device__ __forceinline__ float xrow_max(float x) { return x32_max(x16_max(x)); }
__device__ __forceinline__ float xrow_sum(float x) { return x32_sum(x16_sum(x)); }
__device__ __forceinline__ float row16_max(float x) {
  x = fmaxf(x, dpp<XOR1>(x)); x = fmaxf(x, dpp<XOR2>(x)); x = fmaxf(x, dpp<XOR7>(x)); x = fmaxf(x, dpp<XOR8>(x));
  return x;
}
__device__ __forceinline__ float row16_sum(float x) {
  x += dpp<XOR1>(x); x += dpp<XOR2>(x); x += dpp<XOR7>(x); x += dpp<XOR8>(x);
  return x;
}
__device__ __forceinline__ float bflo(unsigned u) { return __builtin_bit_cast(float, u << 16); }
__device__ __forceinline__ float bfhi(unsigned u) { return __builtin_bit_cast(float, u & 0xffff0000u); }
__device__ __forceinline__ unsigned cvtpk(float lo, float hi) {
  unsigned r; asm volatile("v_cvt_pk_bf16_f32 %0, %1, %2" : "=v"(r) : "v"(lo), "v"(hi)); return r;
}
template <int T> __device__ __forceinline__ float bcast16(float x) { return dpp<0x150 + T>(x); }
__device__ __forceinline__ float ex2(float x) { return __builtin_amdgcn_exp2f(x); }
__device__ __forceinline__ unsigned fbits(float x) { return __builtin_bit_cast(unsigned, x); }
__device__ __forceinline__ float bitsf(unsigned x) { return __builtin_bit_cast(float, x); }

template <typename F, int... Is>
__device__ __forceinline__ void static_for_impl(F&& f, std::integer_sequence<int, Is...>) {
  (f(std::integral_constant<int, Is>{}), ...);
}
template <int N, typename F> __device__ __forceinline__ void static_for(F&& f) {
  static_for_impl(f, std::make_integer_sequence<int, N>{});
}
#define SB() __builtin_amdgcn_sched_barrier(0)
#define PIN8(a) asm volatile("" : "+v"(a[0]), "+v"(a[1]), "+v"(a[2]), "+v"(a[3]), "+v"(a[4]), "+v"(a[5]), "+v"(a[6]), "+v"(a[7]) :: "memory")
#define PIN1(a) asm volatile("" : "+v"(a) :: "memory")

__device__ __forceinline__ void fma8(float (&a)[8], float s, const u32x4& w) {
#pragma unroll
  for (int k = 0; k < 4; ++k) {
    a[2 * k] = fmaf(s, bflo(w[k]), a[2 * k]);
    a[2 * k + 1] = fmaf(s, bfhi(w[k]), a[2 * k + 1]);
  }
}

__device__ __forceinline__ void rows_rs8(const float (&a)[8], float& o0, float& o1) {
  float t[4];
#pragma unroll
  for (int k = 0; k < 4; ++k) {
    auto s = __builtin_amdgcn_permlane32_swap(fbits(a[k]), fbits(a[k + 4]), false, false);
    t[k] = bitsf(s[0]) + bitsf(s[1]);
  }
  auto s0 = __builtin_amdgcn_permlane16_swap(fbits(t[0]), fbits(t[2]), false, false);
  auto s1 = __builtin_amdgcn_permlane16_swap(fbits(t[1]), fbits(t[3]), false, false);
  o0 = bitsf(s0[0]) + bitsf(s0[1]);
  o1 = bitsf(s1[0]) + bitsf(s1[1]);
}

__global__ void __launch_bounds__(NTHR, 2)
attn_fused(const unsigned short* __restrict__ xA, const unsigned short* __restrict__ kA, const unsigned short* __restrict__ vA,
           const unsigned short* __restrict__ wqA, const unsigned short* __restrict__ woA,
           const unsigned short* __restrict__ xB, const unsigned short* __restrict__ kB, const unsigned short* __restrict__ vB,
           const unsigned short* __restrict__ wqB, const unsigned short* __restrict__ woB,
           unsigned char* ws, unsigned short* out) {
  __shared__ __attribute__((aligned(16))) float lds[LDS_FLOATS];
  float* l_q = lds;
  float* l_o = lds + 1024;
  float* l_ml = lds + 2048;
  float* l_y = lds + 2048 + 16;
  float* l_r = lds + 2048 + 16 + 4096;
  STAMP(0);

  const int b = blockIdx.x;
  const int idx = b >> 3;
  const int hg = (b & 7) * 8 + (idx >> 2);
  const int split = idx & 3;
  const int prob = hg >> 5, h = hg & 31;
  const int slot = hg * 4 + split;
  const int tid = threadIdx.x, lane = tid & 63;
  const int wave = __builtin_amdgcn_readfirstlane(tid >> 6);
  const int r = lane >> 4, c = lane & 15;

  const unsigned short* x = prob ? xB : xA;
  const void* kp = prob ? kB : kA;
  const void* vp = prob ? vB : vA;
  const auto rs_q = __builtin_amdgcn_make_buffer_rsrc((void*)(prob ? wqB : wqA), 0, 4u << 20, RSRC_W3);
  const auto rs_o = __builtin_amdgcn_make_buffer_rsrc((void*)(prob ? woB : woA), 0, 4u << 20, RSRC_W3);
  const auto rs_w = __builtin_amdgcn_make_buffer_rsrc((void*)ws, 0, WS_BYTES, RSRC_W3);

  __builtin_amdgcn_raw_buffer_store_b64(u32x2{0u, 0u}, rs_w, tid * 8, WS_Y + slot * 4096, AUX_SC1);
  if (tid < 2) __builtin_amdgcn_raw_buffer_store_b64(u32x2{0u, 0u}, rs_w, tid * 8, WS_ML + slot * 16, AUX_SC1);
  SB();
  const unsigned epoch = *(const __attribute__((address_space(4))) unsigned*)(ws + WS_CTL + 256 * prob);
  const unsigned tag = (epoch + 1u) ? epoch + 1u : 1u;
  const unsigned xr0 = x[wave * 64 + lane];
  SB();

  const int voff_kv = wave * 1024 + lane * 16;
  const int kv_base = (h * SK + split * 2048) * 256;
  auto pair_off = [&](int p) -> int { return kv_base + ((p + split * 4) & (NPO - 1)) * PAIR_B; };

  u32x4 buf[16];
  {
    const int voff_q = (wave * 64 + r * 16) * 8192 + c * 16;
    static_for<16>([&](auto T_) {
      constexpr int T = decltype(T_)::value;
      buf[T] = __builtin_amdgcn_raw_buffer_load_b128(rs_q, voff_q, h * 256 + T * 8192, AUX_NT);
      SB();
    });
    STAMP(1);
    const auto rs_k = __builtin_amdgcn_make_buffer_rsrc((void*)kp, 0, 64u << 20, RSRC_W3);
    const auto rs_v = __builtin_amdgcn_make_buffer_rsrc((void*)vp, 0, 64u << 20, RSRC_W3);
    const int a0 = pair_off(0), a1 = pair_off(1);
    float qa[8];
#pragma unroll
    for (int k = 0; k < 8; ++k) qa[k] = 0.f;
    const float xf0 = bflo(xr0);
    static_for<16>([&](auto T_) {
      constexpr int T = decltype(T_)::value;
      fma8(qa, bcast16<T>(xf0), buf[T]);
      PIN8(qa);
      SB();
      buf[T] = __builtin_amdgcn_raw_buffer_load_b128((T & 4) ? rs_v : rs_k, voff_kv, ((T & 8) ? a1 : a0) + (T & 3) * 8192, AUX_NT);
      SB();
    });
    float q0, q1;
    rows_rs8(qa, q0, q1);
    *(float2*)(l_q + wave * 128 + 8 * c + 2 * r) = make_float2(q0, q1);
    SB();
  }
  __syncthreads();
  u32x4 qp;
  {
    f32x4 a = {0.f, 0.f, 0.f, 0.f}, bb = {0.f, 0.f, 0.f, 0.f};
#pragma unroll
    for (int w = 0; w < NWAVE; ++w) {
      a += *(const f32x4*)(l_q + w * 128 + 8 * c);
      bb += *(const f32x4*)(l_q + w * 128 + 8 * c + 4);
    }
    qp[0] = cvtpk(a[0], a[1]); qp[1] = cvtpk(a[2], a[3]); qp[2] = cvtpk(bb[0], bb[1]); qp[3] = cvtpk(bb[2], bb[3]);
  }
  SB();
  STAMP(2);

  constexpr float C2 = 0.08838834764831845f * 1.4426950408889634f;
  float m = -INFINITY, l = 0.f;
  float acc[8];
#pragma unroll
  for (int k = 0; k < 8; ++k) acc[k] = 0.f;

  auto do_pair = [&](auto S_, int pn, auto&& hook) {
    constexpr int S = decltype(S_)::value;
    const unsigned nrec = pn < NPO ? (64u << 20) : 0u;
    const auto rs_k = __builtin_amdgcn_make_buffer_rsrc((void*)kp, 0, nrec, RSRC_W3);
    const auto rs_v = __builtin_amdgcn_make_buffer_rsrc((void*)vp, 0, nrec, RSRC_W3);
    const int an = pair_off(pn);
    float xs[4];
    static_for<4>([&](auto T_) {
      constexpr int T = decltype(T_)::value;
      const u32x4 kk = buf[S + T];
      float d = dot2(kk[0], qp[0], 0.f);
      d = dot2(kk[1], qp[1], d); d = dot2(kk[2], qp[2], d); d = dot2(kk[3], qp[3], d);
      xs[T] = d;
      PIN1(xs[T]);
      SB();
      buf[S + T] = __builtin_amdgcn_raw_buffer_load_b128(rs_k, voff_kv, an + T * 8192, AUX_NT);
      SB();
      hook(std::integral_constant<int, S + T>{});
      SB();
    });
#pragma unroll
    for (int hh = 0; hh < 2; ++hh) { const bool up = (c & 8); const float keep = up ? xs[hh + 2] : xs[hh], send = up ? xs[hh] : xs[hh + 2]; xs[hh] = keep + dpp<XOR8>(send); }
    { const bool up = (c & 4); const float keep = up ? xs[1] : xs[0], send = up ? xs[0] : xs[1]; xs[0] = keep + dpp<XOR7>(send); }
    xs[0] += dpp<XOR2>(xs[0]);
    xs[0] += dpp<XOR1>(xs[0]);
    const float s = xs[0] * C2;
    float mx = fmaxf(s, dpp<XOR8>(s));
    mx = fmaxf(mx, dpp<XOR7>(mx));
    const float mn = fmaxf(m, mx);
    const float msub = (mn == -INFINITY) ? 0.f : mn;
    const float alpha = ex2(m - msub);
    const float p = ex2(s - msub);
    l = fmaf(l, alpha, p);
    m = mn;
#pragma unroll
    for (int k = 0; k < 8; ++k) acc[k] *= alpha;
    SB();
    static_for<4>([&](auto T_) {
      constexpr int T = decltype(T_)::value;
      fma8(acc, bcast16<4 * T>(p), buf[S + 4 + T]);
      PIN8(acc);
      SB();
      buf[S + 4 + T] = __builtin_amdgcn_raw_buffer_load_b128(rs_v, voff_kv, an + T * 8192, AUX_NT);
      SB();
      hook(std::integral_constant<int, S + 4 + T>{});
      SB();
    });
  };
  float wof[16][8];
  {
    u32x4 wo[16];
    const int voff_o = wave * 16384 + lane * 16;
    auto issue_wo = [&](auto T_) {
      constexpr int T = decltype(T_)::value;
      wo[T] = __builtin_amdgcn_raw_buffer_load_b128(rs_o, voff_o, h * 131072 + T * 1024, AUX_NT);
    };
    auto unpack_wo = [&](auto T_) {
      constexpr int T = decltype(T_)::value;
#pragma unroll
      for (int k = 0; k < 4; ++k) { wof[T][2 * k] = bflo(wo[T][k]); wof[T][2 * k + 1] = bfhi(wo[T][k]); }
      PIN8(wof[T]);
    };
#pragma unroll 1
    for (int it = 0; it < WO_IT; ++it) {
      do_pair(std::integral_constant<int, 0>{}, 2 * it + 2, [](auto) {});
      SB();
      do_pair(std::integral_constant<int, 8>{}, 2 * it + 3, [](auto) {});
      SB();
    }
    do_pair(std::integral_constant<int, 0>{}, 2 * WO_IT + 2, issue_wo);
    SB();
    do_pair(std::integral_constant<int, 8>{}, 2 * WO_IT + 3, issue_wo);
    SB();
    do_pair(std::integral_constant<int, 0>{}, 2 * WO_IT + 4, unpack_wo);
    SB();
    do_pair(std::integral_constant<int, 8>{}, 2 * WO_IT + 5, unpack_wo);
    SB();
  }
#pragma unroll 1
  for (int it = WO_IT + 2; it < NPO / 2; ++it) {
    do_pair(std::integral_constant<int, 0>{}, 2 * it + 2, [](auto) {});
    SB();
    do_pair(std::integral_constant<int, 8>{}, 2 * it + 3, [](auto) {});
  }
  STAMP(4);

  {
    const float Mw = xrow_max(m);
    const float Msub = (Mw == -INFINITY) ? 0.f : Mw;
    const float sc = ex2(m - Msub);
    float lr = l + dpp<XOR8>(l);
    lr += dpp<XOR7>(lr);
    const float Lw = xrow_sum(lr * sc);
#pragma unroll
    for (int k = 0; k < 8; ++k) acc[k] *= sc;
    float o0, o1;
    rows_rs8(acc, o0, o1);
    *(float2*)(l_o + wave * 128 + 8 * c + 2 * r) = make_float2(o0, o1);
    if (lane == 0) *(float2*)(l_ml + 2 * wave) = make_float2(Mw, Lw);
  }
  __syncthreads();
  float M, L, ov;
  {
    const f32x4 ml0 = *(const f32x4*)(l_ml), ml1 = *(const f32x4*)(l_ml + 4), ml2 = *(const f32x4*)(l_ml + 8), ml3 = *(const f32x4*)(l_ml + 12);
    M = fmaxf(fmaxf(fmaxf(ml0[0], ml0[2]), fmaxf(ml1[0], ml1[2])), fmaxf(fmaxf(ml2[0], ml2[2]), fmaxf(ml3[0], ml3[2])));
    const float Ms = (M == -INFINITY) ? 0.f : M;
    const float ma = r == 0 ? ml0[0] : r == 1 ? ml1[0] : r == 2 ? ml2[0] : ml3[0];
    const float la = r == 0 ? ml0[1] : r == 1 ? ml1[1] : r == 2 ? ml2[1] : ml3[1];
    const float mb = r == 0 ? ml0[2] : r == 1 ? ml1[2] : r == 2 ? ml2[2] : ml3[2];
    const float lb = r == 0 ? ml0[3] : r == 1 ? ml1[3] : r == 2 ? ml2[3] : ml3[3];
    const float sa = ex2(ma - Ms), sb = ex2(mb - Ms);
    const int d0 = wave * 16 + c;
    ov = xrow_sum(sa * l_o[(2 * r) * 128 + d0] + sb * l_o[(2 * r + 1) * 128 + d0]);
    L = xrow_sum(sa * la + sb * lb);
  }
  STAMP(5);
  {
    float y[8];
#pragma unroll
    for (int k = 0; k < 8; ++k) y[k] = 0.f;
    static_for<16>([&](auto T_) {
      constexpr int T = decltype(T_)::value;
      const float o = bcast16<T>(ov);
#pragma unroll
      for (int k = 0; k < 8; ++k) y[k] = fmaf(o, wof[T][k], y[k]);
    });
    *(f32x4*)(l_y + wave * 512 + 8 * lane) = f32x4{y[0], y[1], y[2], y[3]};
    *(f32x4*)(l_y + wave * 512 + 8 * lane + 4) = f32x4{y[4], y[5], y[6], y[7]};
  }
  __syncthreads();
  {
    float yv[8];
#pragma unroll
    for (int w = 0; w < 8; ++w) yv[w] = l_y[w * 512 + tid];
    const float y0 = ((yv[0] + yv[1]) + (yv[2] + yv[3])) + ((yv[4] + yv[5]) + (yv[6] + yv[7]));
    __builtin_amdgcn_raw_buffer_store_b64(u32x2{fbits(y0), tag}, rs_w, tid * 8, WS_Y + slot * 4096, AUX_SC1);
    if (tid < 2) __builtin_amdgcn_raw_buffer_store_b64(u32x2{fbits(tid ? L : M), tag}, rs_w, tid * 8, WS_ML + slot * 16, AUX_SC1);
  }
  STAMP(6);

  if (split == 0) {
    const int p0 = prob * 128 + wave * 16 + (lane >> 3);
    const int mloff = WS_ML + p0 * 16;
    const int yoff = WS_Y + p0 * 4096 + (h * 16 + (lane & 7) * 2) * 8;
    u32x4 ml0, ml1, g0, g1;
    ml0 = ml1 = g0 = g1 = u32x4{0u, 0u, 0u, 0u};
#pragma unroll 1
    for (int spin = 0; spin < (1 << 22); ++spin) {
      asm volatile("" ::: "memory");
      const bool need0 = ((ml0[1] ^ tag) | (ml0[3] ^ tag) | (g0[1] ^ tag) | (g0[3] ^ tag)) != 0;
      const bool need1 = ((ml1[1] ^ tag) | (ml1[3] ^ tag) | (g1[1] ^ tag) | (g1[3] ^ tag)) != 0;
      if (!__any(need0 || need1)) break;
      if (need0) {
        ml0 = __builtin_amdgcn_raw_buffer_load_b128(rs_w, mloff, 0, AUX_SC1);
        g0 = __builtin_amdgcn_raw_buffer_load_b128(rs_w, yoff, 0, AUX_SC1);
      }
      if (need1) {
        ml1 = __builtin_amdgcn_raw_buffer_load_b128(rs_w, mloff + 128, 0, AUX_SC1);
        g1 = __builtin_amdgcn_raw_buffer_load_b128(rs_w, yoff + 8 * 4096, 0, AUX_SC1);
      }
    }
    const float m0 = bitsf(ml0[0]), m1 = bitsf(ml1[0]);
    float M0 = fmaxf(m0, dpp<XOR8>(m0)); M0 = x16_max(M0);
    float M1 = fmaxf(m1, dpp<XOR8>(m1)); M1 = x16_max(M1);
    const float e0 = ex2(m0 - M0), e1 = ex2(m1 - M1);
    float L0 = e0 * bitsf(ml0[2]); L0 += dpp<XOR8>(L0); L0 = x16_sum(L0);
    float L1 = e1 * bitsf(ml1[2]); L1 += dpp<XOR8>(L1); L1 = x16_sum(L1);
    const float w0 = e0 / L0, w1 = e1 / L1;
    float v0 = w0 * bitsf(g0[0]) + w1 * bitsf(g1[0]);
    float v1 = w0 * bitsf(g0[2]) + w1 * bitsf(g1[2]);
    v0 += dpp<XOR8>(v0); v0 = xrow_sum(v0);
    v1 += dpp<XOR8>(v1); v1 = xrow_sum(v1);
    if (lane < 8) *(float2*)(l_r + wave * 16 + 2 * lane) = make_float2(v0, v1);
    __syncthreads();
    if (tid < 8) {
      float o0 = 0.f, o1 = 0.f;
#pragma unroll
      for (int w = 0; w < 8; ++w) { const float2 t = *(const float2*)(l_r + w * 16 + 2 * tid); o0 += t.x; o1 += t.y; }
      ((unsigned*)out)[(prob * 512 + h * 16) / 2 + tid] = cvtpk(o0, o1);
    }
    if (h == 0 && tid == 0) __builtin_amdgcn_raw_buffer_store_b32(tag, rs_w, 0, WS_CTL + 256 * prob, AUX_SC1);
  }
  STAMP(3);
}

}

extern "C" void kernel_launch(void* const* d_in, const int* in_sizes, int n_in,
                              void* d_out, int out_size, void* d_ws, size_t ws_size,
                              hipStream_t stream) {
  attn_fused<<<256, NTHR, 0, stream>>>(
      (const unsigned short*)d_in[0], (const unsigned short*)d_in[1], (const unsigned short*)d_in[2],
      (const unsigned short*)d_in[3], (const unsigned short*)d_in[4],
      (const unsigned short*)d_in[5], (const unsigned short*)d_in[6], (const unsigned short*)d_in[7],
      (const unsigned short*)d_in[8], (const unsigned short*)d_in[9], (unsigned char*)d_ws, (unsigned short*)d_out);
}
```

```cpp
#include <hip/hip_runtime.h>
#include <hip/hip_bf16.h>
#include <utility>
#include <type_traits>

#define STAMP(i)

namespace {

typedef unsigned u32x4 __attribute__((ext_vector_type(4)));
typedef unsigned u32x2 __attribute__((ext_vector_type(2)));
typedef float f32x4 __attribute__((ext_vector_type(4)));
typedef __bf16 bf16x2 __attribute__((ext_vector_type(2)));

constexpr int SK = 8192;
constexpr int NWAVE = 8;
constexpr int NTHR = NWAVE * 64;
constexpr int WO_IT = 2;
constexpr int NPO = 16;
constexpr int PAIR_B = 32768;
constexpr int AUX_NT = 2;
constexpr int AUX_SC1 = 16;
constexpr int RSRC_W3 = 0x00020000;

constexpr unsigned WS_CTL = 0;
constexpr unsigned WS_ML = 4096;
constexpr unsigned WS_Y = 65536;
constexpr unsigned WS_BYTES = WS_Y + 256 * 4096;

constexpr int LDS_FLOATS = 21 * 1024;

__device__ __forceinline__ float dot2(unsigned k, unsigned q, float acc) {
  return __builtin_amdgcn_fdot2_f32_bf16(__builtin_bit_cast(bf16x2, k), __builtin_bit_cast(bf16x2, q), acc, false);
}
template <int CTRL> __device__ __forceinline__ float dpp(float x) {
  return __builtin_bit_cast(float, __builtin_amdgcn_mov_dpp(__builtin_bit_cast(int, x), CTRL, 0xf, 0xf, true));
}
constexpr int XOR1 = 0xB1, XOR2 = 0x4E, XOR7 = 0x141, XOR8 = 0x128;
__device__ __forceinline__ float x16_max(float x) {
  auto s = __builtin_amdgcn_permlane16_swap(__float_as_uint(x), __float_as_uint(x), false, false);
  return fmaxf(__uint_as_float(s[0]), __uint_as_float(s[1]));
}
__device__ __forceinline__ float x32_max(float x) {
  auto t = __builtin_amdgcn_permlane32_swap(__float_as_uint(x), __float_as_uint(x), false, false);
  return fmaxf(__uint_as_float(t[0]), __uint_as_float(t[1]));
}
__device__ __forceinline__ float x16_sum(float x) {
  auto s = __builtin_amdgcn_permlane16_swap(__float_as_uint(x), __float_as_uint(x), false, false);
  return __uint_as_float(s[0]) + __uint_as_float(s[1]);
}
__device__ __forceinline__ float x32_sum(float x) {
  auto t = __builtin_amdgcn_permlane32_swap(__float_as_uint(x), __float_as_uint(x), false, false);
  return __uint_as_float(t[0]) + __uint_as_float(t[1]);
}
__device__ __forceinline__ float xrow_max(float x) { return x32_max(x16_max(x)); }
__device__ __forceinline__ float xrow_sum(float x) { return x32_sum(x16_sum(x)); }
__device__ __forceinline__ float row16_max(float x) {
  x = fmaxf(x, dpp<XOR1>(x)); x = fmaxf(x, dpp<XOR2>(x)); x = fmaxf(x, dpp<XOR7>(x)); x = fmaxf(x, dpp<XOR8>(x));
  return x;
}
__device__ __forceinline__ float row16_sum(float x) {
  x += dpp<XOR1>(x); x += dpp<XOR2>(x); x += dpp<XOR7>(x); x += dpp<XOR8>(x);
  return x;
}
__device__ __forceinline__ float bflo(unsigned u) { return __builtin_bit_cast(float, u << 16); }
__device__ __forceinline__ float bfhi(unsigned u) { return __builtin_bit_cast(float, u & 0xffff0000u); }
__device__ __forceinline__ unsigned cvtpk(float lo, float hi) {
  unsigned r; asm volatile("v_cvt_pk_bf16_f32 %0, %1, %2" : "=v"(r) : "v"(lo), "v"(hi)); return r;
}
template <int T> __device__ __forceinline__ float bcast16(float x) { return dpp<0x150 + T>(x); }
__device__ __forceinline__ float ex2(float x) { return __builtin_amdgcn_exp2f(x); }
__device__ __forceinline__ unsigned fbits(float x) { return __builtin_bit_cast(unsigned, x); }
__device__ __forceinline__ float bitsf(unsigned x) { return __builtin_bit_cast(float, x); }

template <typename F, int... Is>
__device__ __forceinline__ void static_for_impl(F&& f, std::integer_sequence<int, Is...>) {
  (f(std::integral_constant<int, Is>{}), ...);
}
template <int N, typename F> __device__ __forceinline__ void static_for(F&& f) {
  static_for_impl(f, std::make_integer_sequence<int, N>{});
}
#define SB() __builtin_amdgcn_sched_barrier(0)
#define PIN8(a) asm volatile("" : "+v"(a[0]), "+v"(a[1]), "+v"(a[2]), "+v"(a[3]), "+v"(a[4]), "+v"(a[5]), "+v"(a[6]), "+v"(a[7]) :: "memory")
#define PIN1(a) asm volatile("" : "+v"(a) :: "memory")

__device__ __forceinline__ void fma8(float (&a)[8], float s, const u32x4& w) {
#pragma unroll
  for (int k = 0; k < 4; ++k) {
    a[2 * k] = fmaf(s, bflo(w[k]), a[2 * k]);
    a[2 * k + 1] = fmaf(s, bfhi(w[k]), a[2 * k + 1]);
  }
}

__device__ __forceinline__ void rows_rs8(const float (&a)[8], float& o0, float& o1) {
  float t[4];
#pragma unroll
  for (int k = 0; k < 4; ++k) {
    auto s = __builtin_amdgcn_permlane32_swap(fbits(a[k]), fbits(a[k + 4]), false, false);
    t[k] = bitsf(s[0]) + bitsf(s[1]);
  }
  auto s0 = __builtin_amdgcn_permlane16_swap(fbits(t[0]), fbits(t[2]), false, false);
  auto s1 = __builtin_amdgcn_permlane16_swap(fbits(t[1]), fbits(t[3]), false, false);
  o0 = bitsf(s0[0]) + bitsf(s0[1]);
  o1 = bitsf(s1[0]) + bitsf(s1[1]);
}

__global__ void __launch_bounds__(NTHR, 2)
attn_fused(const unsigned short* __restrict__ xA, const unsigned short* __restrict__ kA, const unsigned short* __restrict__ vA,
           const unsigned short* __restrict__ wqA, const unsigned short* __restrict__ woA,
           const unsigned short* __restrict__ xB, const unsigned short* __restrict__ kB, const unsigned short* __restrict__ vB,
           const unsigned short* __restrict__ wqB, const unsigned short* __restrict__ woB,
           unsigned char* ws, unsigned short* out) {
  __shared__ __attribute__((aligned(16))) float lds[LDS_FLOATS];
  float* l_q = lds;
  float* l_o = lds + 1024;
  float* l_ml = lds + 2048;
  float* l_y = lds + 2048 + 16;
  float* l_r = lds + 2048 + 16 + 4096;
  STAMP(0);

  const int b = blockIdx.x;
  const int idx = b >> 3;
  const int xq = b & 7, jq = idx >> 2;
  const int hg = jq * 8 + ((xq + jq) & 7);
  const int split = idx & 3;
  const int prob = hg >> 5, h = hg & 31;
  const int slot = hg * 4 + split;
  const int tid = threadIdx.x, lane = tid & 63;
  const int wave = __builtin_amdgcn_readfirstlane(tid >> 6);
  const int r = lane >> 4, c = lane & 15;

  const unsigned short* x = prob ? xB : xA;
  const void* kp = prob ? kB : kA;
  const void* vp = prob ? vB : vA;
  const auto rs_q = __builtin_amdgcn_make_buffer_rsrc((void*)(prob ? wqB : wqA), 0, 4u << 20, RSRC_W3);
  const auto rs_o = __builtin_amdgcn_make_buffer_rsrc((void*)(prob ? woB : woA), 0, 4u << 20, RSRC_W3);
  const auto rs_w = __builtin_amdgcn_make_buffer_rsrc((void*)ws, 0, WS_BYTES, RSRC_W3);

  __builtin_amdgcn_raw_buffer_store_b64(u32x2{0u, 0u}, rs_w, tid * 8, WS_Y + slot * 4096, AUX_SC1);
  if (tid < 2) __builtin_amdgcn_raw_buffer_store_b64(u32x2{0u, 0u}, rs_w, tid * 8, WS_ML + slot * 16, AUX_SC1);
  SB();
  const unsigned epoch = *(const __attribute__((address_space(4))) unsigned*)(ws + WS_CTL + 256 * prob);
  const unsigned xr0 = x[wave * 64 + lane];
  SB();

  const int voff_kv = wave * 4096 + lane * 16;
  const int kv_base = (h * SK + split * 2048) * 256;
  const int rot = split * 4;
  auto pair_off = [&](int p) -> int { return kv_base + ((p + rot) & (NPO - 1)) * PAIR_B; };

  unsigned tag;
  u32x4 buf[16];
  {
    const int voff_q = (wave * 64 + r * 16) * 8192 + c * 16;
    static_for<16>([&](auto T_) {
      constexpr int T = decltype(T_)::value;
      buf[T] = __builtin_amdgcn_raw_buffer_load_b128(rs_q, voff_q, h * 256 + T * 8192, AUX_NT);
      SB();
    });
    STAMP(1);
    tag = epoch;
    asm volatile("" : "+s"(tag));
    tag = (tag + 1u) ? tag + 1u : 1u;
    const auto rs_k = __builtin_amdgcn_make_buffer_rsrc((void*)kp, 0, 64u << 20, RSRC_W3);
    const auto rs_v = __builtin_amdgcn_make_buffer_rsrc((void*)vp, 0, 64u << 20, RSRC_W3);
    const int a0 = pair_off(0), a1 = pair_off(1);
    float qa[8];
#pragma unroll
    for (int k = 0; k < 8; ++k) qa[k] = 0.f;
    const float xf0 = bflo(xr0);
    static_for<16>([&](auto T_) {
      constexpr int T = decltype(T_)::value;
      fma8(qa, bcast16<T>(xf0), buf[T]);
      PIN8(qa);
      SB();
      buf[T] = __builtin_amdgcn_raw_buffer_load_b128((T & 4) ? rs_v : rs_k, voff_kv, ((T & 8) ? a1 : a0) + (T & 3) * 1024, AUX_NT);
      SB();
    });
    float q0, q1;
    rows_rs8(qa, q0, q1);
    *(float2*)(l_q + wave * 128 + 8 * c + 2 * r) = make_float2(q0, q1);
    SB();
  }
  __syncthreads();
  u32x4 qp;
  {
    f32x4 a = {0.f, 0.f, 0.f, 0.f}, bb = {0.f, 0.f, 0.f, 0.f};
#pragma unroll
    for (int w = 0; w < NWAVE; ++w) {
      a += *(const f32x4*)(l_q + w * 128 + 8 * c);
      bb += *(const f32x4*)(l_q + w * 128 + 8 * c + 4);
    }
    qp[0] = cvtpk(a[0], a[1]); qp[1] = cvtpk(a[2], a[3]); qp[2] = cvtpk(bb[0], bb[1]); qp[3] = cvtpk(bb[2], bb[3]);
  }
  SB();
  STAMP(2);

  constexpr float C2 = 0.08838834764831845f * 1.4426950408889634f;
  float m = -INFINITY, l = 0.f;
  float acc[8];
#pragma unroll
  for (int k = 0; k < 8; ++k) acc[k] = 0.f;

  auto do_pair = [&](auto S_, int pn, auto&& hook) {
    constexpr int S = decltype(S_)::value;
    const unsigned nrec = pn < NPO ? (64u << 20) : 0u;
    const auto rs_k = __builtin_amdgcn_make_buffer_rsrc((void*)kp, 0, nrec, RSRC_W3);
    const auto rs_v = __builtin_amdgcn_make_buffer_rsrc((void*)vp, 0, nrec, RSRC_W3);
    const int an = pair_off(pn);
    float xs[4];
    static_for<4>([&](auto T_) {
      constexpr int T = decltype(T_)::value;
      const u32x4 kk = buf[S + T];
      float d = dot2(kk[0], qp[0], 0.f);
      d = dot2(kk[1], qp[1], d); d = dot2(kk[2], qp[2], d); d = dot2(kk[3], qp[3], d);
      xs[T] = d;
      PIN1(xs[T]);
      SB();
      buf[S + T] = __builtin_amdgcn_raw_buffer_load_b128(rs_k, voff_kv, an + T * 1024, AUX_NT);
      SB();
      hook(std::integral_constant<int, S + T>{});
      SB();
    });
#pragma unroll
    for (int hh = 0; hh < 2; ++hh) { const bool up = (c & 8); const float keep = up ? xs[hh + 2] : xs[hh], send = up ? xs[hh] : xs[hh + 2]; xs[hh] = keep + dpp<XOR8>(send); }
    { const bool up = (c & 4); const float keep = up ? xs[1] : xs[0], send = up ? xs[0] : xs[1]; xs[0] = keep + dpp<XOR7>(send); }
    xs[0] += dpp<XOR2>(xs[0]);
    xs[0] += dpp<XOR1>(xs[0]);
    const float s = xs[0] * C2;
    float mx = fmaxf(s, dpp<XOR8>(s));
    mx = fmaxf(mx, dpp<XOR7>(mx));
    const float mn = fmaxf(m, mx);
    const float msub = (mn == -INFINITY) ? 0.f : mn;
    const float alpha = ex2(m - msub);
    const float p = ex2(s - msub);
    l = fmaf(l, alpha, p);
    m = mn;
#pragma unroll
    for (int k = 0; k < 8; ++k) acc[k] *= alpha;
    SB();
    static_for<4>([&](auto T_) {
      constexpr int T = decltype(T_)::value;
      fma8(acc, bcast16<4 * T>(p), buf[S + 4 + T]);
      PIN8(acc);
      SB();
      buf[S + 4 + T] = __builtin_amdgcn_raw_buffer_load_b128(rs_v, voff_kv, an + T * 1024, AUX_NT);
      SB();
      hook(std::integral_constant<int, S + 4 + T>{});
      SB();
    });
  };
  float wof[16][8];
  {
    u32x4 wo[16];
    const int voff_o = wave * 16384 + lane * 16;
    auto issue_wo = [&](auto T_) {
      constexpr int T = decltype(T_)::value;
      wo[T] = __builtin_amdgcn_raw_buffer_load_b128(rs_o, voff_o, h * 131072 + T * 1024, AUX_NT);
    };
    auto unpack_wo = [&](auto T_) {
      constexpr int T = decltype(T_)::value;
#pragma unroll
      for (int k = 0; k < 4; ++k) { wof[T][2 * k] = bflo(wo[T][k]); wof[T][2 * k + 1] = bfhi(wo[T][k]); }
      PIN8(wof[T]);
    };
#pragma unroll 1
    for (int it = 0; it < WO_IT; ++it) {
      do_pair(std::integral_constant<int, 0>{}, 2 * it + 2, [](auto) {});
      SB();
      do_pair(std::integral_constant<int, 8>{}, 2 * it + 3, [](auto) {});
      SB();
    }
    do_pair(std::integral_constant<int, 0>{}, 2 * WO_IT + 2, issue_wo);
    SB();
    do_pair(std::integral_constant<int, 8>{}, 2 * WO_IT + 3, issue_wo);
    SB();
    do_pair(std::integral_constant<int, 0>{}, 2 * WO_IT + 4, unpack_wo);
    SB();
    do_pair(std::integral_constant<int, 8>{}, 2 * WO_IT + 5, unpack_wo);
    SB();
  }
#pragma unroll 1
  for (int it = WO_IT + 2; it < NPO / 2; ++it) {
    do_pair(std::integral_constant<int, 0>{}, 2 * it + 2, [](auto) {});
    SB();
    do_pair(std::integral_constant<int, 8>{}, 2 * it + 3, [](auto) {});
  }
  STAMP(4);

  {
    const float Mw = xrow_max(m);
    const float Msub = (Mw == -INFINITY) ? 0.f : Mw;
    const float sc = ex2(m - Msub);
    float lr = l + dpp<XOR8>(l);
    lr += dpp<XOR7>(lr);
    const float Lw = xrow_sum(lr * sc);
#pragma unroll
    for (int k = 0; k < 8; ++k) acc[k] *= sc;
    float o0, o1;
    rows_rs8(acc, o0, o1);
    *(float2*)(l_o + wave * 128 + 8 * c + 2 * r) = make_float2(o0, o1);
    if (lane == 0) *(float2*)(l_ml + 2 * wave) = make_float2(Mw, Lw);
  }
  __syncthreads();
  float M, L, ov;
  {
    const f32x4 ml = *(const f32x4*)(l_ml + 4 * r);
    const float oa = l_o[(2 * r) * 128 + wave * 16 + c], ob = l_o[(2 * r + 1) * 128 + wave * 16 + c];
    M = xrow_max(fmaxf(ml[0], ml[2]));
    const float Ms = (M == -INFINITY) ? 0.f : M;
    const float sa = ex2(ml[0] - Ms), sb = ex2(ml[2] - Ms);
    ov = xrow_sum(fmaf(sa, oa, sb * ob));
    L = xrow_sum(fmaf(sa, ml[1], sb * ml[3]));
  }
  STAMP(5);
  {
    float y[8];
#pragma unroll
    for (int k = 0; k < 8; ++k) y[k] = 0.f;
    static_for<16>([&](auto T_) {
      constexpr int T = decltype(T_)::value;
      const float o = bcast16<T>(ov);
#pragma unroll
      for (int k = 0; k < 8; ++k) y[k] = fmaf(o, wof[T][k], y[k]);
    });
    *(f32x4*)(l_y + wave * 512 + 8 * lane) = f32x4{y[0], y[1], y[2], y[3]};
    *(f32x4*)(l_y + wave * 512 + 8 * lane + 4) = f32x4{y[4], y[5], y[6], y[7]};
  }
  __syncthreads();
  {
    float yv[8];
#pragma unroll
    for (int w = 0; w < 8; ++w) yv[w] = l_y[w * 512 + tid];
    const float y0 = ((yv[0] + yv[1]) + (yv[2] + yv[3])) + ((yv[4] + yv[5]) + (yv[6] + yv[7]));
    __builtin_amdgcn_raw_buffer_store_b64(u32x2{fbits(y0), tag}, rs_w, tid * 8, WS_Y + slot * 4096, AUX_SC1);
    if (tid < 2) __builtin_amdgcn_raw_buffer_store_b64(u32x2{fbits(tid ? L : M), tag}, rs_w, tid * 8, WS_ML + slot * 16, AUX_SC1);
  }
  STAMP(6);

  if (split == 0) {
    const int p0 = prob * 128 + wave * 16 + (lane >> 3);
    const int mloff = WS_ML + p0 * 16;
    const int yoff = WS_Y + p0 * 4096 + (h * 16 + (lane & 7) * 2) * 8;
    u32x4 ml0, ml1, g0, g1;
    ml0 = ml1 = g0 = g1 = u32x4{0u, 0u, 0u, 0u};
#pragma unroll 1
    for (int spin = 0; spin < (1 << 22); ++spin) {
      asm volatile("" ::: "memory");
      const bool need0 = ((ml0[1] ^ tag) | (ml0[3] ^ tag) | (g0[1] ^ tag) | (g0[3] ^ tag)) != 0;
      const bool need1 = ((ml1[1] ^ tag) | (ml1[3] ^ tag) | (g1[1] ^ tag) | (g1[3] ^ tag)) != 0;
      if (!__any(need0 || need1)) break;
      if (need0) {
        ml0 = __builtin_amdgcn_raw_buffer_load_b128(rs_w, mloff, 0, AUX_SC1);
        g0 = __builtin_amdgcn_raw_buffer_load_b128(rs_w, yoff, 0, AUX_SC1);
      }
      if (need1) {
        ml1 = __builtin_amdgcn_raw_buffer_load_b128(rs_w, mloff + 128, 0, AUX_SC1);
        g1 = __builtin_amdgcn_raw_buffer_load_b128(rs_w, yoff + 8 * 4096, 0, AUX_SC1);
      }
    }
    const float m0 = bitsf(ml0[0]), m1 = bitsf(ml1[0]);
    float M0 = fmaxf(m0, dpp<XOR8>(m0)); M0 = x16_max(M0);
    float M1 = fmaxf(m1, dpp<XOR8>(m1)); M1 = x16_max(M1);
    const float e0 = ex2(m0 - M0), e1 = ex2(m1 - M1);
    float L0 = e0 * bitsf(ml0[2]); L0 += dpp<XOR8>(L0); L0 = x16_sum(L0);
    float L1 = e1 * bitsf(ml1[2]); L1 += dpp<XOR8>(L1); L1 = x16_sum(L1);
    const float w0 = e0 / L0, w1 = e1 / L1;
    float v0 = w0 * bitsf(g0[0]) + w1 * bitsf(g1[0]);
    float v1 = w0 * bitsf(g0[2]) + w1 * bitsf(g1[2]);
    v0 += dpp<XOR8>(v0); v0 = xrow_sum(v0);
    v1 += dpp<XOR8>(v1); v1 = xrow_sum(v1);
    if (lane < 8) *(float2*)(l_r + wave * 16 + 2 * lane) = make_float2(v0, v1);
    __syncthreads();
    if (tid < 8) {
      float o0 = 0.f, o1 = 0.f;
#pragma unroll
      for (int w = 0; w < 8; ++w) { const float2 t = *(const float2*)(l_r + w * 16 + 2 * tid); o0 += t.x; o1 += t.y; }
      ((unsigned*)out)[(prob * 512 + h * 16) / 2 + tid] = cvtpk(o0, o1);
    }
    if (h == 0 && tid == 0) __builtin_amdgcn_raw_buffer_store_b32(tag, rs_w, 0, WS_CTL + 256 * prob, AUX_SC1);
  }
  STAMP(3);
}

}

extern "C" void kernel_launch(void* const* d_in, const int* in_sizes, int n_in,
                              void* d_out, int out_size, void* d_ws, size_t ws_size,
                              hipStream_t stream) {
  attn_fused<<<256, NTHR, 0, stream>>>(
      (const unsigned short*)d_in[0], (const unsigned short*)d_in[1], (const unsigned short*)d_in[2],
      (const unsigned short*)d_in[3], (const unsigned short*)d_in[4],
      (const unsigned short*)d_in[5], (const unsigned short*)d_in[6], (const unsigned short*)d_in[7],
      (const unsigned short*)d_in[8], (const unsigned short*)d_in[9], (unsigned char*)d_ws, (unsigned short*)d_out);
}
```

```cpp
#include <hip/hip_runtime.h>
#include <hip/hip_bf16.h>
#include <utility>
#include <type_traits>

namespace {

typedef unsigned u32x4 __attribute__((ext_vector_type(4)));
typedef unsigned u32x2 __attribute__((ext_vector_type(2)));
typedef float f32x4 __attribute__((ext_vector_type(4)));
typedef __bf16 bf16x2 __attribute__((ext_vector_type(2)));

constexpr int SK = 8192;
constexpr int NWAVE = 8;
constexpr int NTHR = NWAVE * 64;
constexpr int WO_IT = 3;
constexpr int NPO = 16;
constexpr int PAIR_B = 32768;
constexpr int AUX_NT = 2;
constexpr int AUX_SC1 = 16;
constexpr int RSRC_W3 = 0x00020000;

constexpr unsigned WS_CTL = 0;
constexpr unsigned WS_ML = 4096;
constexpr unsigned WS_Y = 65536;
constexpr unsigned WS_BYTES = WS_Y + 256 * 4096;

constexpr int LDS_FLOATS = 21 * 1024;

__device__ __forceinline__ float dot2(unsigned k, unsigned q, float acc) {
  return __builtin_amdgcn_fdot2_f32_bf16(__builtin_bit_cast(bf16x2, k), __builtin_bit_cast(bf16x2, q), acc, false);
}
template <int CTRL> __device__ __forceinline__ float dpp(float x) {
  return __builtin_bit_cast(float, __builtin_amdgcn_mov_dpp(__builtin_bit_cast(int, x), CTRL, 0xf, 0xf, true));
}
constexpr int XOR1 = 0xB1, XOR2 = 0x4E, XOR7 = 0x141, XOR8 = 0x128;
__device__ __forceinline__ float x16_max(float x) {
  auto s = __builtin_amdgcn_permlane16_swap(__float_as_uint(x), __float_as_uint(x), false, false);
  return fmaxf(__uint_as_float(s[0]), __uint_as_float(s[1]));
}
__device__ __forceinline__ float x32_max(float x) {
  auto t = __builtin_amdgcn_permlane32_swap(__float_as_uint(x), __float_as_uint(x), false, false);
  return fmaxf(__uint_as_float(t[0]), __uint_as_float(t[1]));
}
__device__ __forceinline__ float x16_sum(float x) {
  auto s = __builtin_amdgcn_permlane16_swap(__float_as_uint(x), __float_as_uint(x), false, false);
  return __uint_as_float(s[0]) + __uint_as_float(s[1]);
}
__device__ __forceinline__ float x32_sum(float x) {
  auto t = __builtin_amdgcn_permlane32_swap(__float_as_uint(x), __float_as_uint(x), false, false);
  return __uint_as_float(t[0]) + __uint_as_float(t[1]);
}
__device__ __forceinline__ float xrow_max(float x) { return x32_max(x16_max(x)); }
__device__ __forceinline__ float xrow_sum(float x) { return x32_sum(x16_sum(x)); }
__device__ __forceinline__ float bflo(unsigned u) { return __builtin_bit_cast(float, u << 16); }
__device__ __forceinline__ float bfhi(unsigned u) { return __builtin_bit_cast(float, u & 0xffff0000u); }
__device__ __forceinline__ unsigned cvtpk(float lo, float hi) {
  unsigned r; asm volatile("v_cvt_pk_bf16_f32 %0, %1, %2" : "=v"(r) : "v"(lo), "v"(hi)); return r;
}
template <int T> __device__ __forceinline__ float bcast16(float x) { return dpp<0x150 + T>(x); }
__device__ __forceinline__ float ex2(float x) { return __builtin_amdgcn_exp2f(x); }
__device__ __forceinline__ unsigned fbits(float x) { return __builtin_bit_cast(unsigned, x); }
__device__ __forceinline__ float bitsf(unsigned x) { return __builtin_bit_cast(float, x); }

template <typename F, int... Is>
__device__ __forceinline__ void static_for_impl(F&& f, std::integer_sequence<int, Is...>) {
  (f(std::integral_constant<int, Is>{}), ...);
}
template <int N, typename F> __device__ __forceinline__ void static_for(F&& f) {
  static_for_impl(f, std::make_integer_sequence<int, N>{});
}
#define SB() __builtin_amdgcn_sched_barrier(0)
#define PIN8(a) asm volatile("" : "+v"(a[0]), "+v"(a[1]), "+v"(a[2]), "+v"(a[3]), "+v"(a[4]), "+v"(a[5]), "+v"(a[6]), "+v"(a[7]) :: "memory")
#define PIN1(a) asm volatile("" : "+v"(a) :: "memory")

__device__ __forceinline__ void fma8(float (&a)[8], float s, const u32x4& w) {
#pragma unroll
  for (int k = 0; k < 4; ++k) {
    a[2 * k] = fmaf(s, bflo(w[k]), a[2 * k]);
    a[2 * k + 1] = fmaf(s, bfhi(w[k]), a[2 * k + 1]);
  }
}


__device__ __forceinline__ void rows_rs8(const float (&a)[8], float& o0, float& o1) {
  float t[4];
#pragma unroll
  for (int k = 0; k < 4; ++k) {
    auto s = __builtin_amdgcn_permlane32_swap(fbits(a[k]), fbits(a[k + 4]), false, false);
    t[k] = bitsf(s[0]) + bitsf(s[1]);
  }
  auto s0 = __builtin_amdgcn_permlane16_swap(fbits(t[0]), fbits(t[2]), false, false);
  auto s1 = __builtin_amdgcn_permlane16_swap(fbits(t[1]), fbits(t[3]), false, false);
  o0 = bitsf(s0[0]) + bitsf(s0[1]);
  o1 = bitsf(s1[0]) + bitsf(s1[1]);
}

__global__ void __launch_bounds__(NTHR, 2)
attn_fused(const unsigned short* __restrict__ xA, const unsigned short* __restrict__ kA, const unsigned short* __restrict__ vA,
           const unsigned short* __restrict__ wqA, const unsigned short* __restrict__ woA,
           const unsigned short* __restrict__ xB, const unsigned short* __restrict__ kB, const unsigned short* __restrict__ vB,
           const unsigned short* __restrict__ wqB, const unsigned short* __restrict__ woB,
           unsigned char* ws, unsigned short* out) {
  __shared__ __attribute__((aligned(16))) float lds[LDS_FLOATS];
  float* l_q = lds;
  float* l_o = lds + 1024;
  float* l_ml = lds + 2048;
  float* l_y = lds + 2048 + 16;
  float* l_r = lds + 2048 + 16 + 4096;

  const int b = blockIdx.x;
  const int idx = b >> 3;
  const int xq = b & 7, jq = idx >> 2;
  const int hg = jq * 8 + ((xq + jq) & 7);
  const int split = idx & 3;
  const int prob = hg >> 5, h = hg & 31;
  const int slot = hg * 4 + split;
  const int tid = threadIdx.x, lane = tid & 63;
  const int wave = __builtin_amdgcn_readfirstlane(tid >> 6);
  const int r = lane >> 4, c = lane & 15;

  const unsigned short* x = prob ? xB : xA;
  const void* kp = prob ? kB : kA;
  const void* vp = prob ? vB : vA;
  const auto rs_q = __builtin_amdgcn_make_buffer_rsrc((void*)(prob ? wqB : wqA), 0, 4u << 20, RSRC_W3);
  const auto rs_o = __builtin_amdgcn_make_buffer_rsrc((void*)(prob ? woB : woA), 0, 4u << 20, RSRC_W3);
  const auto rs_w = __builtin_amdgcn_make_buffer_rsrc((void*)ws, 0, WS_BYTES, RSRC_W3);

  __builtin_amdgcn_raw_buffer_store_b64(u32x2{0u, 0u}, rs_w, tid * 8, WS_Y + slot * 4096, AUX_SC1);
  if (tid < 2) __builtin_amdgcn_raw_buffer_store_b64(u32x2{0u, 0u}, rs_w, tid * 8, WS_ML + slot * 16, AUX_SC1);
  SB();
  const unsigned epoch = *(const __attribute__((address_space(4))) unsigned*)(ws + WS_CTL + 256 * prob);
  const unsigned xr0 = x[wave * 64 + lane];
  SB();

  const int voff_kv = wave * 4096 + lane * 16;
  const int kv_base = (h * SK + split * 2048) * 256;
  const int rot = split * 4;
  auto pair_off = [&](int p) -> int { return kv_base + ((p + rot) & (NPO - 1)) * PAIR_B; };

  unsigned tag;
  u32x4 buf[16];
  {
    const int voff_q = (wave * 64 + r * 16) * 8192 + c * 16;
    static_for<16>([&](auto T_) {
      constexpr int T = decltype(T_)::value;
      buf[T] = __builtin_amdgcn_raw_buffer_load_b128(rs_q, voff_q, h * 256 + T * 8192, AUX_NT);
      SB();
    });
    tag = epoch;
    asm volatile("" : "+s"(tag));
    tag = (tag + 1u) ? tag + 1u : 1u;
    const auto rs_k = __builtin_amdgcn_make_buffer_rsrc((void*)kp, 0, 64u << 20, RSRC_W3);
    const auto rs_v = __builtin_amdgcn_make_buffer_rsrc((void*)vp, 0, 64u << 20, RSRC_W3);
    const int a0 = pair_off(0), a1 = pair_off(1);
    float qa[8];
#pragma unroll
    for (int k = 0; k < 8; ++k) qa[k] = 0.f;
    const float xf0 = bflo(xr0);
    static_for<16>([&](auto T_) {
      constexpr int T = decltype(T_)::value;
      fma8(qa, bcast16<T>(xf0), buf[T]);
      PIN8(qa);
      SB();
      buf[T] = __builtin_amdgcn_raw_buffer_load_b128((T & 4) ? rs_v : rs_k, voff_kv, ((T & 8) ? a1 : a0) + (T & 3) * 1024, AUX_NT);
      SB();
    });
    float q0, q1;
    rows_rs8(qa, q0, q1);
    *(float2*)(l_q + wave * 128 + 8 * c + 2 * r) = make_float2(q0, q1);
    SB();
  }
  __syncthreads();
  u32x4 qp;
  {
    f32x4 a = {0.f, 0.f, 0.f, 0.f}, bb = {0.f, 0.f, 0.f, 0.f};
#pragma unroll
    for (int w = 0; w < NWAVE; ++w) {
      a += *(const f32x4*)(l_q + w * 128 + 8 * c);
      bb += *(const f32x4*)(l_q + w * 128 + 8 * c + 4);
    }
    qp[0] = cvtpk(a[0], a[1]); qp[1] = cvtpk(a[2], a[3]); qp[2] = cvtpk(bb[0], bb[1]); qp[3] = cvtpk(bb[2], bb[3]);
  }
  SB();

  constexpr float C2 = 0.08838834764831845f * 1.4426950408889634f;
  float m = -INFINITY, l = 0.f;
  float acc[8];
#pragma unroll
  for (int k = 0; k < 8; ++k) acc[k] = 0.f;

  auto do_pair = [&](auto S_, int pn, auto&& hook) {
    constexpr int S = decltype(S_)::value;
    const unsigned nrec = pn < NPO ? (64u << 20) : 0u;
    const auto rs_k = __builtin_amdgcn_make_buffer_rsrc((void*)kp, 0, nrec, RSRC_W3);
    const auto rs_v = __builtin_amdgcn_make_buffer_rsrc((void*)vp, 0, nrec, RSRC_W3);
    const int an = pair_off(pn);
    float xs[4];
    static_for<4>([&](auto T_) {
      constexpr int T = decltype(T_)::value;
      const u32x4 kk = buf[S + T];
      float d = dot2(kk[0], qp[0], 0.f);
      d = dot2(kk[1], qp[1], d); d = dot2(kk[2], qp[2], d); d = dot2(kk[3], qp[3], d);
      xs[T] = d;
      PIN1(xs[T]);
      SB();
      buf[S + T] = __builtin_amdgcn_raw_buffer_load_b128(rs_k, voff_kv, an + T * 1024, AUX_NT);
      SB();
      hook(std::integral_constant<int, S + T>{});
      SB();
    });
#pragma unroll
    for (int hh = 0; hh < 2; ++hh) { const bool up = (c & 8); const float keep = up ? xs[hh + 2] : xs[hh], send = up ? xs[hh] : xs[hh + 2]; xs[hh] = keep + dpp<XOR8>(send); }
    { const bool up = (c & 4); const float keep = up ? xs[1] : xs[0], send = up ? xs[0] : xs[1]; xs[0] = keep + dpp<XOR7>(send); }
    xs[0] += dpp<XOR2>(xs[0]);
    xs[0] += dpp<XOR1>(xs[0]);
    const float s = xs[0] * C2;
    float mx = fmaxf(s, dpp<XOR8>(s));
    mx = fmaxf(mx, dpp<XOR7>(mx));
    const float mn = fmaxf(m, mx);
    const float msub = (mn == -INFINITY) ? 0.f : mn;
    const float alpha = ex2(m - msub);
    const float p = ex2(s - msub);
    l = fmaf(l, alpha, p);
    m = mn;
#pragma unroll
    for (int k = 0; k < 8; ++k) acc[k] *= alpha;
    SB();
    static_for<4>([&](auto T_) {
      constexpr int T = decltype(T_)::value;
      fma8(acc, bcast16<4 * T>(p), buf[S + 4 + T]);
      PIN8(acc);
      SB();
      buf[S + 4 + T] = __builtin_amdgcn_raw_buffer_load_b128(rs_v, voff_kv, an + T * 1024, AUX_NT);
      SB();
      hook(std::integral_constant<int, S + 4 + T>{});
      SB();
    });
  };
  float wof[16][8];
  {
    u32x4 wo[16];
    const int voff_o = wave * 16384 + lane * 16;
    auto issue_wo = [&](auto T_) {
      constexpr int T = decltype(T_)::value;
      wo[T] = __builtin_amdgcn_raw_buffer_load_b128(rs_o, voff_o, h * 131072 + T * 1024, AUX_NT);
    };
    auto unpack_wo = [&](auto T_) {
      constexpr int T = decltype(T_)::value;
#pragma unroll
      for (int k = 0; k < 4; ++k) { wof[T][2 * k] = bflo(wo[T][k]); wof[T][2 * k + 1] = bfhi(wo[T][k]); }
      PIN8(wof[T]);
    };
#pragma unroll 1
    for (int it = 0; it < WO_IT; ++it) {
      do_pair(std::integral_constant<int, 0>{}, 2 * it + 2, [](auto) {});
      SB();
      do_pair(std::integral_constant<int, 8>{}, 2 * it + 3, [](auto) {});
      SB();
    }
    do_pair(std::integral_constant<int, 0>{}, 2 * WO_IT + 2, issue_wo);
    SB();
    do_pair(std::integral_constant<int, 8>{}, 2 * WO_IT + 3, issue_wo);
    SB();
    do_pair(std::integral_constant<int, 0>{}, 2 * WO_IT + 4, unpack_wo);
    SB();
    do_pair(std::integral_constant<int, 8>{}, 2 * WO_IT + 5, unpack_wo);
    SB();
  }
#pragma unroll 1
  for (int it = WO_IT + 2; it < NPO / 2; ++it) {
    do_pair(std::integral_constant<int, 0>{}, 2 * it + 2, [](auto) {});
    SB();
    do_pair(std::integral_constant<int, 8>{}, 2 * it + 3, [](auto) {});
  }

  {
    const float Mw = xrow_max(m);
    const float Msub = (Mw == -INFINITY) ? 0.f : Mw;
    const float sc = ex2(m - Msub);
    float lr = l + dpp<XOR8>(l);
    lr += dpp<XOR7>(lr);
    const float Lw = xrow_sum(lr * sc);
#pragma unroll
    for (int k = 0; k < 8; ++k) acc[k] *= sc;
    float o0, o1;
    rows_rs8(acc, o0, o1);
    *(float2*)(l_o + wave * 128 + 8 * c + 2 * r) = make_float2(o0, o1);
    if (lane == 0) *(float2*)(l_ml + 2 * wave) = make_float2(Mw, Lw);
  }
  __syncthreads();
  float M, L, ov;
  {
    const f32x4 ml = *(const f32x4*)(l_ml + 4 * r);
    const float oa = l_o[(2 * r) * 128 + wave * 16 + c], ob = l_o[(2 * r + 1) * 128 + wave * 16 + c];
    M = xrow_max(fmaxf(ml[0], ml[2]));
    const float Ms = (M == -INFINITY) ? 0.f : M;
    const float sa = ex2(ml[0] - Ms), sb = ex2(ml[2] - Ms);
    ov = xrow_sum(fmaf(sa, oa, sb * ob));
    L = xrow_sum(fmaf(sa, ml[1], sb * ml[3]));
  }
  {
    float y[8];
#pragma unroll
    for (int k = 0; k < 8; ++k) y[k] = 0.f;
    static_for<16>([&](auto T_) {
      constexpr int T = decltype(T_)::value;
      const float o = bcast16<T>(ov);
#pragma unroll
      for (int k = 0; k < 8; ++k) y[k] = fmaf(o, wof[T][k], y[k]);
    });
    *(f32x4*)(l_y + wave * 512 + 8 * lane) = f32x4{y[0], y[1], y[2], y[3]};
    *(f32x4*)(l_y + wave * 512 + 8 * lane + 4) = f32x4{y[4], y[5], y[6], y[7]};
  }
  __syncthreads();
  {
    float yv[8];
#pragma unroll
    for (int w = 0; w < 8; ++w) yv[w] = l_y[w * 512 + tid];
    const float y0 = ((yv[0] + yv[1]) + (yv[2] + yv[3])) + ((yv[4] + yv[5]) + (yv[6] + yv[7]));
    __builtin_amdgcn_raw_buffer_store_b64(u32x2{fbits(y0), tag}, rs_w, tid * 8, WS_Y + slot * 4096, AUX_SC1);
    if (tid < 2) __builtin_amdgcn_raw_buffer_store_b64(u32x2{fbits(tid ? L : M), tag}, rs_w, tid * 8, WS_ML + slot * 16, AUX_SC1);
  }

  if (split == 0) {
    const int p0 = prob * 128 + wave * 16 + (lane >> 3);
    const int mloff = WS_ML + p0 * 16;
    const int yoff = WS_Y + p0 * 4096 + (h * 16 + (lane & 7) * 2) * 8;
    u32x4 ml0, ml1, g0, g1;
    ml0 = ml1 = g0 = g1 = u32x4{0u, 0u, 0u, 0u};
#pragma unroll 1
    for (int spin = 0; spin < (1 << 22); ++spin) {
      asm volatile("" ::: "memory");
      const bool need0 = ((ml0[1] ^ tag) | (ml0[3] ^ tag) | (g0[1] ^ tag) | (g0[3] ^ tag)) != 0;
      const bool need1 = ((ml1[1] ^ tag) | (ml1[3] ^ tag) | (g1[1] ^ tag) | (g1[3] ^ tag)) != 0;
      if (!__any(need0 || need1)) break;
      if (need0) {
        ml0 = __builtin_amdgcn_raw_buffer_load_b128(rs_w, mloff, 0, AUX_SC1);
        g0 = __builtin_amdgcn_raw_buffer_load_b128(rs_w, yoff, 0, AUX_SC1);
      }
      if (need1) {
        ml1 = __builtin_amdgcn_raw_buffer_load_b128(rs_w, mloff + 128, 0, AUX_SC1);
        g1 = __builtin_amdgcn_raw_buffer_load_b128(rs_w, yoff + 8 * 4096, 0, AUX_SC1);
      }
    }
    const float m0 = bitsf(ml0[0]), m1 = bitsf(ml1[0]);
    float M0 = fmaxf(m0, dpp<XOR8>(m0)); M0 = x16_max(M0);
    float M1 = fmaxf(m1, dpp<XOR8>(m1)); M1 = x16_max(M1);
    const float e0 = ex2(m0 - M0), e1 = ex2(m1 - M1);
    float L0 = e0 * bitsf(ml0[2]); L0 += dpp<XOR8>(L0); L0 = x16_sum(L0);
    float L1 = e1 * bitsf(ml1[2]); L1 += dpp<XOR8>(L1); L1 = x16_sum(L1);
    const float w0 = e0 / L0, w1 = e1 / L1;
    float v0 = w0 * bitsf(g0[0]) + w1 * bitsf(g1[0]);
    float v1 = w0 * bitsf(g0[2]) + w1 * bitsf(g1[2]);
    v0 += dpp<XOR8>(v0); v0 = xrow_sum(v0);
    v1 += dpp<XOR8>(v1); v1 = xrow_sum(v1);
    if (lane < 8) *(float2*)(l_r + wave * 16 + 2 * lane) = make_float2(v0, v1);
    __syncthreads();
    if (tid < 8) {
      float o0 = 0.f, o1 = 0.f;
#pragma unroll
      for (int w = 0; w < 8; ++w) { const float2 t = *(const float2*)(l_r + w * 16 + 2 * tid); o0 += t.x; o1 += t.y; }
      ((unsigned*)out)[(prob * 512 + h * 16) / 2 + tid] = cvtpk(o0, o1);
    }
    if (h == 0 && tid == 0) __builtin_amdgcn_raw_buffer_store_b32(tag, rs_w, 0, WS_CTL + 256 * prob, AUX_SC1);
  }
}

}

extern "C" void kernel_launch(void* const* d_in, const int* in_sizes, int n_in,
                              void* d_out, int out_size, void* d_ws, size_t ws_size,
                              hipStream_t stream) {
  attn_fused<<<256, NTHR, 0, stream>>>(
      (const unsigned short*)d_in[0], (const unsigned short*)d_in[1], (const unsigned short*)d_in[2],
      (const unsigned short*)d_in[3], (const unsigned short*)d_in[4],
      (const unsigned short*)d_in[5], (const unsigned short*)d_in[6], (const unsigned short*)d_in[7],
      (const unsigned short*)d_in[8], (const unsigned short*)d_in[9], (unsigned char*)d_ws, (unsigned short*)d_out);
}
```

```cpp
#include <hip/hip_runtime.h>
#include <hip/hip_bf16.h>
#include <utility>
#include <type_traits>

namespace {

typedef unsigned u32x4 __attribute__((ext_vector_type(4)));
typedef unsigned u32x2 __attribute__((ext_vector_type(2)));
typedef float f32x4 __attribute__((ext_vector_type(4)));
typedef __bf16 bf16x2 __attribute__((ext_vector_type(2)));

constexpr int SK = 8192;
constexpr int NWAVE = 8;
constexpr int NTHR = NWAVE * 64;
constexpr int WO_IT = 3;
constexpr int NPO = 16;
constexpr int PAIR_B = 32768;
constexpr int AUX_NT = 2;
constexpr int AUX_SC1 = 16;
constexpr int RSRC_W3 = 0x00020000;

constexpr unsigned WS_CTL = 0;
constexpr unsigned WS_ML = 4096;
constexpr unsigned WS_Y = 65536;
constexpr unsigned WS_BYTES = WS_Y + 256 * 4096;

constexpr int LDS_FLOATS = 21 * 1024;

__device__ __forceinline__ float dot2(unsigned k, unsigned q, float acc) {
  return __builtin_amdgcn_fdot2_f32_bf16(__builtin_bit_cast(bf16x2, k), __builtin_bit_cast(bf16x2, q), acc, false);
}
template <int CTRL> __device__ __forceinline__ float dpp(float x) {
  return __builtin_bit_cast(float, __builtin_amdgcn_mov_dpp(__builtin_bit_cast(int, x), CTRL, 0xf, 0xf, true));
}
constexpr int XOR1 = 0xB1, XOR2 = 0x4E, XOR7 = 0x141, XOR8 = 0x128;
__device__ __forceinline__ float x16_max(float x) {
  auto s = __builtin_amdgcn_permlane16_swap(__float_as_uint(x), __float_as_uint(x), false, false);
  return fmaxf(__uint_as_float(s[0]), __uint_as_float(s[1]));
}
__device__ __forceinline__ float x32_max(float x) {
  auto t = __builtin_amdgcn_permlane32_swap(__float_as_uint(x), __float_as_uint(x), false, false);
  return fmaxf(__uint_as_float(t[0]), __uint_as_float(t[1]));
}
__device__ __forceinline__ float x16_sum(float x) {
  auto s = __builtin_amdgcn_permlane16_swap(__float_as_uint(x), __float_as_uint(x), false, false);
  return __uint_as_float(s[0]) + __uint_as_float(s[1]);
}
__device__ __forceinline__ float x32_sum(float x) {
  auto t = __builtin_amdgcn_permlane32_swap(__float_as_uint(x), __float_as_uint(x), false, false);
  return __uint_as_float(t[0]) + __uint_as_float(t[1]);
}
__device__ __forceinline__ float xrow_max(float x) { return x32_max(x16_max(x)); }
__device__ __forceinline__ float xrow_sum(float x) { return x32_sum(x16_sum(x)); }
__device__ __forceinline__ float bflo(unsigned u) { return __builtin_bit_cast(float, u << 16); }
__device__ __forceinline__ float bfhi(unsigned u) { return __builtin_bit_cast(float, u & 0xffff0000u); }
__device__ __forceinline__ unsigned cvtpk(float lo, float hi) {
  unsigned r; asm volatile("v_cvt_pk_bf16_f32 %0, %1, %2" : "=v"(r) : "v"(lo), "v"(hi)); return r;
}
template <int T> __device__ __forceinline__ float bcast16(float x) { return dpp<0x150 + T>(x); }
__device__ __forceinline__ float ex2(float x) { return __builtin_amdgcn_exp2f(x); }
__device__ __forceinline__ unsigned fbits(float x) { return __builtin_bit_cast(unsigned, x); }
__device__ __forceinline__ float bitsf(unsigned x) { return __builtin_bit_cast(float, x); }

template <typename F, int... Is>
__device__ __forceinline__ void static_for_impl(F&& f, std::integer_sequence<int, Is...>) {
  (f(std::integral_constant<int, Is>{}), ...);
}
template <int N, typename F> __device__ __forceinline__ void static_for(F&& f) {
  static_for_impl(f, std::make_integer_sequence<int, N>{});
}
#define SB() __builtin_amdgcn_sched_barrier(0)
#define PIN8(a) asm volatile("" : "+v"(a[0]), "+v"(a[1]), "+v"(a[2]), "+v"(a[3]), "+v"(a[4]), "+v"(a[5]), "+v"(a[6]), "+v"(a[7]) :: "memory")
#define PIN1(a) asm volatile("" : "+v"(a) :: "memory")

__device__ __forceinline__ void fma8(float (&a)[8], float s, const u32x4& w) {
#pragma unroll
  for (int k = 0; k < 4; ++k) {
    a[2 * k] = fmaf(s, bflo(w[k]), a[2 * k]);
    a[2 * k + 1] = fmaf(s, bfhi(w[k]), a[2 * k + 1]);
  }
}


__device__ __forceinline__ void rows_rs8(const float (&a)[8], float& o0, float& o1) {
  float t[4];
#pragma unroll
  for (int k = 0; k < 4; ++k) {
    auto s = __builtin_amdgcn_permlane32_swap(fbits(a[k]), fbits(a[k + 4]), false, false);
    t[k] = bitsf(s[0]) + bitsf(s[1]);
  }
  auto s0 = __builtin_amdgcn_permlane16_swap(fbits(t[0]), fbits(t[2]), false, false);
  auto s1 = __builtin_amdgcn_permlane16_swap(fbits(t[1]), fbits(t[3]), false, false);
  o0 = bitsf(s0[0]) + bitsf(s0[1]);
  o1 = bitsf(s1[0]) + bitsf(s1[1]);
}

__global__ void __launch_bounds__(NTHR, 2)
attn_fused(const unsigned short* __restrict__ xA, const unsigned short* __restrict__ kA, const unsigned short* __restrict__ vA,
           const unsigned short* __restrict__ wqA, const unsigned short* __restrict__ woA,
           const unsigned short* __restrict__ xB, const unsigned short* __restrict__ kB, const unsigned short* __restrict__ vB,
           const unsigned short* __restrict__ wqB, const unsigned short* __restrict__ woB,
           unsigned char* ws, unsigned short* out) {
  __shared__ __attribute__((aligned(16))) float lds[LDS_FLOATS];
  float* l_q = lds;
  float* l_o = lds + 1024;
  float* l_ml = lds + 2048;
  float* l_y = lds + 2048 + 16;
  float* l_r = lds + 2048 + 16 + 4096;

  const int b = blockIdx.x;
  const int idx = b >> 3;
  const int xq = b & 7, jq = idx >> 2;
  const int hg = jq * 8 + ((xq + jq) & 7);
  const int split = idx & 3;
  const int prob = hg >> 5, h = hg & 31;
  const int slot = hg * 4 + split;
  const int tid = threadIdx.x, lane = tid & 63;
  const int wave = __builtin_amdgcn_readfirstlane(tid >> 6);
  const int r = lane >> 4, c = lane & 15;

  const unsigned short* x = prob ? xB : xA;
  const void* kp = prob ? kB : kA;
  const void* vp = prob ? vB : vA;
  const auto rs_q = __builtin_amdgcn_make_buffer_rsrc((void*)(prob ? wqB : wqA), 0, 4u << 20, RSRC_W3);
  const auto rs_o = __builtin_amdgcn_make_buffer_rsrc((void*)(prob ? woB : woA), 0, 4u << 20, RSRC_W3);
  const auto rs_w = __builtin_amdgcn_make_buffer_rsrc((void*)ws, 0, WS_BYTES, RSRC_W3);

  __builtin_amdgcn_raw_buffer_store_b64(u32x2{0u, 0u}, rs_w, tid * 8, WS_Y + slot * 4096, AUX_SC1);
  if (tid < 2) __builtin_amdgcn_raw_buffer_store_b64(u32x2{0u, 0u}, rs_w, tid * 8, WS_ML + slot * 16, AUX_SC1);
  SB();
  const unsigned epoch = *(const __attribute__((address_space(4))) unsigned*)(ws + WS_CTL + 256 * prob);
  const unsigned xr0 = x[wave * 64 + lane];
  SB();

  const int voff_kv = wave * 4096 + lane * 16;
  const int kv_base = (h * SK + split * 2048) * 256;
  const int rot = split * 4;
  auto pair_off = [&](int p) -> int { return kv_base + ((p + rot) & (NPO - 1)) * PAIR_B; };

  unsigned tag;
  u32x4 buf[16];
  {
    const int voff_q = (wave * 64 + r * 16) * 8192 + c * 16;
    static_for<16>([&](auto T_) {
      constexpr int T = decltype(T_)::value;
      buf[T] = __builtin_amdgcn_raw_buffer_load_b128(rs_q, voff_q, h * 256 + T * 8192, AUX_NT);
      SB();
    });
    tag = epoch;
    asm volatile("" : "+s"(tag));
    tag = (tag + 1u) ? tag + 1u : 1u;
    const auto rs_k = __builtin_amdgcn_make_buffer_rsrc((void*)kp, 0, 64u << 20, RSRC_W3);
    const auto rs_v = __builtin_amdgcn_make_buffer_rsrc((void*)vp, 0, 64u << 20, RSRC_W3);
    const int a0 = pair_off(0), a1 = pair_off(1);
    float qa[8];
#pragma unroll
    for (int k = 0; k < 8; ++k) qa[k] = 0.f;
    const float xf0 = bflo(xr0);
    static_for<16>([&](auto T_) {
      constexpr int T = decltype(T_)::value;
      fma8(qa, bcast16<T>(xf0), buf[T]);
      PIN8(qa);
      SB();
      buf[T] = __builtin_amdgcn_raw_buffer_load_b128((T & 4) ? rs_v : rs_k, voff_kv, ((T & 8) ? a1 : a0) + (T & 3) * 1024, AUX_NT);
      SB();
    });
    float q0, q1;
    rows_rs8(qa, q0, q1);
    *(float2*)(l_q + wave * 128 + 8 * c + 2 * r) = make_float2(q0, q1);
    SB();
  }
  __syncthreads();
  u32x4 qp;
  {
    f32x4 a = {0.f, 0.f, 0.f, 0.f}, bb = {0.f, 0.f, 0.f, 0.f};
#pragma unroll
    for (int w = 0; w < NWAVE; ++w) {
      a += *(const f32x4*)(l_q + w * 128 + 8 * c);
      bb += *(const f32x4*)(l_q + w * 128 + 8 * c + 4);
    }
    qp[0] = cvtpk(a[0], a[1]); qp[1] = cvtpk(a[2], a[3]); qp[2] = cvtpk(bb[0], bb[1]); qp[3] = cvtpk(bb[2], bb[3]);
  }
  SB();

  constexpr float C2 = 0.08838834764831845f * 1.4426950408889634f;
  float m = -INFINITY, l = 0.f;
  float acc[8];
#pragma unroll
  for (int k = 0; k < 8; ++k) acc[k] = 0.f;

  float Mw = 0.f, Lw = 0.f;
  auto do_pair = [&](auto S_, int pn, auto&& hook) {
    constexpr int S = decltype(S_)::value & 8;
    constexpr int MODE = decltype(S_)::value >> 4;
    const auto rs_k = __builtin_amdgcn_make_buffer_rsrc((void*)kp, 0, 64u << 20, RSRC_W3);
    const auto rs_v = __builtin_amdgcn_make_buffer_rsrc((void*)vp, 0, 64u << 20, RSRC_W3);
    const int an = pair_off(pn);
    float xs[4];
    static_for<4>([&](auto T_) {
      constexpr int T = decltype(T_)::value;
      const u32x4 kk = buf[S + T];
      float d = dot2(kk[0], qp[0], 0.f);
      d = dot2(kk[1], qp[1], d); d = dot2(kk[2], qp[2], d); d = dot2(kk[3], qp[3], d);
      xs[T] = d;
      PIN1(xs[T]);
      SB();
      if constexpr (MODE == 0) buf[S + T] = __builtin_amdgcn_raw_buffer_load_b128(rs_k, voff_kv, an + T * 1024, AUX_NT);
      SB();
      hook(std::integral_constant<int, S + T>{});
      SB();
    });
#pragma unroll
    for (int hh = 0; hh < 2; ++hh) { const bool up = (c & 8); const float keep = up ? xs[hh + 2] : xs[hh], send = up ? xs[hh] : xs[hh + 2]; xs[hh] = keep + dpp<XOR8>(send); }
    { const bool up = (c & 4); const float keep = up ? xs[1] : xs[0], send = up ? xs[0] : xs[1]; xs[0] = keep + dpp<XOR7>(send); }
    xs[0] += dpp<XOR2>(xs[0]);
    xs[0] += dpp<XOR1>(xs[0]);
    const float s = xs[0] * C2;
    float mx = fmaxf(s, dpp<XOR8>(s));
    mx = fmaxf(mx, dpp<XOR7>(mx));
    float mn = fmaxf(m, mx);
    if constexpr (MODE == 2) mn = xrow_max(mn);
    const float msub = (mn == -INFINITY) ? 0.f : mn;
    const float alpha = ex2(m - msub);
    const float p = ex2(s - msub);
    l = fmaf(l, alpha, p);
    m = mn;
    if constexpr (MODE == 2) {
      float lr = l + dpp<XOR8>(l);
      lr += dpp<XOR7>(lr);
      Mw = mn;
      Lw = xrow_sum(lr);
      if (lane == 0) *(float2*)(l_ml + 2 * wave) = make_float2(Mw, Lw);
    }
#pragma unroll
    for (int k = 0; k < 8; ++k) acc[k] *= alpha;
    SB();
    static_for<4>([&](auto T_) {
      constexpr int T = decltype(T_)::value;
      fma8(acc, bcast16<4 * T>(p), buf[S + 4 + T]);
      PIN8(acc);
      SB();
      if constexpr (MODE == 0) buf[S + 4 + T] = __builtin_amdgcn_raw_buffer_load_b128(rs_v, voff_kv, an + T * 1024, AUX_NT);
      SB();
      hook(std::integral_constant<int, S + 4 + T>{});
      SB();
    });
  };
  float wof[16][8];
  {
    u32x4 wo[16];
    const int voff_o = wave * 16384 + lane * 16;
    auto issue_wo = [&](auto T_) {
      constexpr int T = decltype(T_)::value;
      wo[T] = __builtin_amdgcn_raw_buffer_load_b128(rs_o, voff_o, h * 131072 + T * 1024, AUX_NT);
    };
    auto unpack_wo = [&](auto T_) {
      constexpr int T = decltype(T_)::value;
#pragma unroll
      for (int k = 0; k < 4; ++k) { wof[T][2 * k] = bflo(wo[T][k]); wof[T][2 * k + 1] = bfhi(wo[T][k]); }
      PIN8(wof[T]);
    };
#pragma unroll 1
    for (int it = 0; it < WO_IT; ++it) {
      do_pair(std::integral_constant<int, 0>{}, 2 * it + 2, [](auto) {});
      SB();
      do_pair(std::integral_constant<int, 8>{}, 2 * it + 3, [](auto) {});
      SB();
    }
    do_pair(std::integral_constant<int, 0>{}, 2 * WO_IT + 2, issue_wo);
    SB();
    do_pair(std::integral_constant<int, 8>{}, 2 * WO_IT + 3, issue_wo);
    SB();
    do_pair(std::integral_constant<int, 0>{}, 2 * WO_IT + 4, unpack_wo);
    SB();
    do_pair(std::integral_constant<int, 8>{}, 2 * WO_IT + 5, unpack_wo);
    SB();
  }
#pragma unroll 1
  for (int it = WO_IT + 2; it < NPO / 2 - 1; ++it) {
    do_pair(std::integral_constant<int, 0>{}, 2 * it + 2, [](auto) {});
    SB();
    do_pair(std::integral_constant<int, 8>{}, 2 * it + 3, [](auto) {});
  }
  SB();
  do_pair(std::integral_constant<int, 16 + 0>{}, 0, [](auto) {});
  SB();
  do_pair(std::integral_constant<int, 32 + 8>{}, 0, [](auto) {});

  {
    float o0, o1;
    rows_rs8(acc, o0, o1);
    *(float2*)(l_o + wave * 128 + 8 * c + 2 * r) = make_float2(o0, o1);
  }
  __syncthreads();
  float M, L, ov;
  {
    const f32x4 ml = *(const f32x4*)(l_ml + 4 * r);
    const float oa = l_o[(2 * r) * 128 + wave * 16 + c], ob = l_o[(2 * r + 1) * 128 + wave * 16 + c];
    M = xrow_max(fmaxf(ml[0], ml[2]));
    const float Ms = (M == -INFINITY) ? 0.f : M;
    const float sa = ex2(ml[0] - Ms), sb = ex2(ml[2] - Ms);
    ov = xrow_sum(fmaf(sa, oa, sb * ob));
    L = xrow_sum(fmaf(sa, ml[1], sb * ml[3]));
  }
  if (tid < 2) __builtin_amdgcn_raw_buffer_store_b64(u32x2{fbits(tid ? L : M), tag}, rs_w, tid * 8, WS_ML + slot * 16, AUX_SC1);
  {
    float y[8];
#pragma unroll
    for (int k = 0; k < 8; ++k) y[k] = 0.f;
    static_for<16>([&](auto T_) {
      constexpr int T = decltype(T_)::value;
      const float o = bcast16<T>(ov);
#pragma unroll
      for (int k = 0; k < 8; ++k) y[k] = fmaf(o, wof[T][k], y[k]);
    });
    *(f32x4*)(l_y + wave * 512 + 8 * lane) = f32x4{y[0], y[1], y[2], y[3]};
    *(f32x4*)(l_y + wave * 512 + 8 * lane + 4) = f32x4{y[4], y[5], y[6], y[7]};
  }
  __syncthreads();
  {
    float yv[8];
#pragma unroll
    for (int w = 0; w < 8; ++w) yv[w] = l_y[w * 512 + tid];
    const float y0 = ((yv[0] + yv[1]) + (yv[2] + yv[3])) + ((yv[4] + yv[5]) + (yv[6] + yv[7]));
    __builtin_amdgcn_raw_buffer_store_b64(u32x2{fbits(y0), tag}, rs_w, tid * 8, WS_Y + slot * 4096, AUX_SC1);
  }

  if (split == 0) {
    const int p0 = prob * 128 + wave * 16 + (lane >> 3);
    const int mloff = WS_ML + p0 * 16;
    const int yoff = WS_Y + p0 * 4096 + (h * 16 + (lane & 7) * 2) * 8;
    u32x4 ml0, ml1, g0, g1;
    ml0 = ml1 = g0 = g1 = u32x4{0u, 0u, 0u, 0u};
    auto bad = [&](const u32x4& v) { return ((v[1] ^ tag) | (v[3] ^ tag)) != 0; };
#pragma unroll 1
    for (int spin = 0; spin < (1 << 22); ++spin) {
      asm volatile("" ::: "memory");
      const bool nm0 = bad(ml0), nm1 = bad(ml1);
      if (!__any(nm0 || nm1)) break;
      if (nm0) ml0 = __builtin_amdgcn_raw_buffer_load_b128(rs_w, mloff, 0, AUX_SC1);
      if (nm1) ml1 = __builtin_amdgcn_raw_buffer_load_b128(rs_w, mloff + 128, 0, AUX_SC1);
      if (bad(g0)) g0 = __builtin_amdgcn_raw_buffer_load_b128(rs_w, yoff, 0, AUX_SC1);
      if (bad(g1)) g1 = __builtin_amdgcn_raw_buffer_load_b128(rs_w, yoff + 8 * 4096, 0, AUX_SC1);
    }
    const float m0 = bitsf(ml0[0]), m1 = bitsf(ml1[0]);
    float M0 = fmaxf(m0, dpp<XOR8>(m0)); M0 = x16_max(M0);
    float M1 = fmaxf(m1, dpp<XOR8>(m1)); M1 = x16_max(M1);
    const float e0 = ex2(m0 - M0), e1 = ex2(m1 - M1);
    float L0 = e0 * bitsf(ml0[2]); L0 += dpp<XOR8>(L0); L0 = x16_sum(L0);
    float L1 = e1 * bitsf(ml1[2]); L1 += dpp<XOR8>(L1); L1 = x16_sum(L1);
    float w0 = e0 / L0, w1 = e1 / L1;
    asm volatile("" : "+v"(w0), "+v"(w1));
#pragma unroll 1
    for (int spin = 0; spin < (1 << 22); ++spin) {
      asm volatile("" ::: "memory");
      const bool n0 = bad(g0), n1 = bad(g1);
      if (!__any(n0 || n1)) break;
      if (n0) g0 = __builtin_amdgcn_raw_buffer_load_b128(rs_w, yoff, 0, AUX_SC1);
      if (n1) g1 = __builtin_amdgcn_raw_buffer_load_b128(rs_w, yoff + 8 * 4096, 0, AUX_SC1);
    }
    float v0 = w0 * bitsf(g0[0]) + w1 * bitsf(g1[0]);
    float v1 = w0 * bitsf(g0[2]) + w1 * bitsf(g1[2]);
    auto sw = __builtin_amdgcn_permlane32_swap(fbits(v0), fbits(v1), false, false);
    float vv = bitsf(sw[0]) + bitsf(sw[1]);
    vv += dpp<XOR8>(vv);
    vv = x16_sum(vv);
    if ((lane & 31) < 8) l_r[wave * 16 + 2 * (lane & 7) + (lane >> 5)] = vv;
    __syncthreads();
    if (wave == 0) {
      const float2 t = *(const float2*)(l_r + (lane >> 3) * 16 + 2 * (lane & 7));
      auto s2 = __builtin_amdgcn_permlane32_swap(fbits(t.x), fbits(t.y), false, false);
      float u = bitsf(s2[0]) + bitsf(s2[1]);
      u += dpp<XOR8>(u);
      u = x16_sum(u);
      auto s3 = __builtin_amdgcn_permlane32_swap(fbits(u), fbits(u), false, false);
      if (lane < 8) ((unsigned*)out)[(prob * 512 + h * 16) / 2 + lane] = cvtpk(bitsf(s3[0]), bitsf(s3[1]));
    }
    if (h == 0 && tid == 0) __builtin_amdgcn_raw_buffer_store_b32(tag, rs_w, 0, WS_CTL + 256 * prob, AUX_SC1);
  }
}

}

extern "C" void kernel_launch(void* const* d_in, const int* in_sizes, int n_in,
                              void* d_out, int out_size, void* d_ws, size_t ws_size,
                              hipStream_t stream) {
  attn_fused<<<256, NTHR, 0, stream>>>(
      (const unsigned short*)d_in[0], (const unsigned short*)d_in[1], (const unsigned short*)d_in[2],
      (const unsigned short*)d_in[3], (const unsigned short*)d_in[4],
      (const unsigned short*)d_in[5], (const unsigned short*)d_in[6], (const unsigned short*)d_in[7],
      (const unsigned short*)d_in[8], (const unsigned short*)d_in[9], (unsigned char*)d_ws, (unsigned short*)d_out);
}
```
